```python
import torch

def _softmax(x, axis=-1):
    return torch.softmax(x, dim=axis)
def _gelu(x):
    return torch.nn.functional.gelu(x, approximate="tanh")
def _silu(x):
    return torch.nn.functional.silu(x)
def _sigmoid(x):
    return torch.sigmoid(x)
def _layernorm(x, axis=-1, eps=1e-5):
    m = x.mean(dim=axis, keepdim=True)
    v = x.var(dim=axis, keepdim=True, unbiased=False)
    return (x - m) / torch.sqrt(v + eps)
def _scatter_add(tgt, idx, src, axis=0):
    return tgt.clone().index_add_(axis, idx, src)
def _segment_sum(src, seg, n):
    out = torch.zeros((n,) + src.shape[1:], dtype=src.dtype)
    return out.index_add_(0, seg, src)
def _pad(x, pw):
    flat = tuple(v for pair in reversed(pw) for v in pair)
    return torch.nn.functional.pad(x, flat)

kernel_name = 'interleave_layers__sliding_block_cached__mixer_channel_mlp__decode'

def setup_inputs():

    _w = (lambda a: (lambda t: t.to(torch.bfloat16) if t.is_floating_point() else t)(torch.as_tensor(a)))
    def _A_setup():
        B, Sq, D = 1, 1, 448
        Hq, Hkv, Sk, Dh = 16, 8, 65536, 256
        assert Hq % Hkv == 0, (Hq, Hkv)
        return {
            "x": _w((lambda *s: torch.randn(*s, dtype=torch.bfloat16) * 0.05)(B, Sq, D)),
            "k_cache": _w((lambda *s: torch.randn(*s, dtype=torch.bfloat16) * 3.2)(B, Hkv, Sk, Dh)),
            "v_cache": _w((lambda *s: torch.randn(*s, dtype=torch.bfloat16) * 3.2)(B, Hkv, Sk, Dh)),
            "wq": _w((lambda *s: torch.randn(*s, dtype=torch.bfloat16) * 3.2)(D, Hq * Dh) * (D ** -0.5)),
            "wo": _w((lambda *s: torch.randn(*s, dtype=torch.bfloat16) * 3.2)(Hq * Dh, D) * ((Hq * Dh) ** -0.5)),
        }
    _A_in = _A_setup()

    def _B_setup():
        B, S, D, F = 1, 1, 448, 1792
        return {
            "x": _w((lambda *s: torch.randn(*s, dtype=torch.bfloat16) * 0.05)(B, S, D)),
            "w1": _w((lambda *s: torch.randn(*s, dtype=torch.bfloat16) * 3.2)(D, F) * (D ** -0.5)),
            "b1": _w((lambda *s: torch.randn(*s, dtype=torch.bfloat16) * 3.2)(F)),
            "w2": _w((lambda *s: torch.randn(*s, dtype=torch.bfloat16) * 3.2)(F, D) * (F ** -0.5)),
            "b2": _w((lambda *s: torch.randn(*s, dtype=torch.bfloat16) * 3.2)(D)),
        }
    _B_in = _B_setup()

    return {
        "A_x": _A_in["x"],
        "A_k_cache": _A_in["k_cache"],
        "A_v_cache": _A_in["v_cache"],
        "A_wq": _A_in["wq"],
        "A_wo": _A_in["wo"],
        "B_x": _B_in["x"],
        "B_w1": _B_in["w1"],
        "B_b1": _B_in["b1"],
        "B_w2": _B_in["w2"],
        "B_b2": _B_in["b2"],
    }

def reference(A_x, A_k_cache, A_v_cache, A_wq, A_wo, B_x, B_w1, B_b1, B_w2, B_b2):
    def _A_ref(x, k_cache, v_cache, wq, wo):
        B, Sq, D = x.shape
        Hq, Dh = 16, 256
        Hkv, Sk = k_cache.shape[1], k_cache.shape[2]
        W = min(4096, max(256, Sk // 4))
        rep = Hq // Hkv
        q = (lambda x, *d: x.permute(*d))((x @ wq).reshape(B, Sq, Hq, Dh), 0, 2, 1, 3)
        k = (lambda x, n, axis: torch.repeat_interleave(x, n, dim=axis))(k_cache[:, :, Sk - W:, :], rep, axis=1)
        v = (lambda x, n, axis: torch.repeat_interleave(x, n, dim=axis))(v_cache[:, :, Sk - W:, :], rep, axis=1)
        scores = torch.einsum("bhqd,bhkd->bhqk", q, k) * (Dh ** -0.5)
        p = _softmax(scores, axis=-1)
        o = torch.einsum("bhqk,bhkd->bhqd", p, v)
        return (lambda x, *d: x.permute(*d))(o, 0, 2, 1, 3).reshape(B, Sq, Hq * Dh) @ wo

    def _B_ref(x, w1, b1, w2, b2):
        y = _layernorm(x, axis=-1)
        h = _gelu(torch.einsum("bsd,df->bsf", y, w1) + b1)
        return torch.einsum("bsf,fd->bsd", h, w2) + b2

    x = A_x
    x = x + _A_ref(_layernorm(x, axis=-1), A_k_cache, A_v_cache, A_wq, A_wo)
    x = x + _B_ref(_layernorm(x, axis=-1), B_w1, B_b1, B_w2, B_b2)
    x = x + _A_ref(_layernorm(x, axis=-1), A_k_cache, A_v_cache, A_wq, A_wo)
    x = x + _B_ref(_layernorm(x, axis=-1), B_w1, B_b1, B_w2, B_b2)
    return x
```

```cpp
#include <hip/hip_runtime.h>
#include <hip/hip_bf16.h>
#include <stdint.h>

typedef unsigned short u16;
__device__ __forceinline__ float bf2f(u16 v) { return __uint_as_float(((unsigned)v) << 16); }
__device__ __forceinline__ float rbf(float f) { return f; }
__device__ __forceinline__ u16 f2bf(float f) { __hip_bfloat16 b = __float2bfloat16(f); return *(u16*)&b; }

constexpr int D = 448, HQ = 16, HKV = 8, SK = 65536, DH = 256, W = 4096, F = 1792;

__device__ void ln_block(const float* xin, float* yout, float* red) {
    int t = threadIdx.x;
    float s = 0.f;
    for (int i = t; i < D; i += 256) s += xin[i];
    for (int o = 32; o > 0; o >>= 1) s += __shfl_xor(s, o);
    __syncthreads();
    if ((t & 63) == 0) red[t >> 6] = s;
    __syncthreads();
    float mean = (red[0] + red[1] + red[2] + red[3]) * (1.0f / D);
    float v = 0.f;
    for (int i = t; i < D; i += 256) { float d = xin[i] - mean; v += d * d; }
    for (int o = 32; o > 0; o >>= 1) v += __shfl_xor(v, o);
    __syncthreads();
    if ((t & 63) == 0) red[t >> 6] = v;
    __syncthreads();
    float var = (red[0] + red[1] + red[2] + red[3]) * (1.0f / D);
    float mb = rbf(mean), vb = rbf(var);
    float den = rbf(sqrtf(rbf(vb + 1e-5f)));
    for (int i = t; i < D; i += 256) yout[i] = rbf(rbf(xin[i] - mb) / den);
    __syncthreads();
}

__global__ void k_q(const u16* x, const u16* wq, float* q) {
    __shared__ float xs[D], ys[D], red[4];
    int t = threadIdx.x;
    for (int i = t; i < D; i += 256) xs[i] = bf2f(x[i]);
    __syncthreads();
    ln_block(xs, ys, red);
    int col = blockIdx.x * 256 + t;
    float acc = 0.f;
    for (int k = 0; k < D; ++k) acc += ys[k] * bf2f(wq[(size_t)k * 4096 + col]);
    q[col] = rbf(acc);
}

__global__ void k_attn(const float* q, const u16* kc, const u16* vc, float* part  ) {
    __shared__ float qs[512], sc[256], red[8];
    int t = threadIdx.x, b = blockIdx.x, h = b >> 5, c = b & 31;
    qs[t] = q[h * 512 + t]; qs[t + 256] = q[h * 512 + 256 + t];
    __syncthreads();
    int hd = t >> 7, key = t & 127;
    const u16* kp = kc + ((size_t)h * SK + (SK - W) + c * 128 + key) * DH;
    float acc = 0.f;
    for (int d = 0; d < DH; ++d) acc += qs[hd * 256 + d] * bf2f(kp[d]);
    float s = rbf(rbf(acc) * 0.0625f);
    float m = s;
    for (int o = 32; o > 0; o >>= 1) m = fmaxf(m, __shfl_xor(m, o));
    if ((t & 63) == 0) red[t >> 6] = m;
    __syncthreads();
    m = fmaxf(red[hd * 2], red[hd * 2 + 1]);
    float p = __expf(s - m);
    sc[t] = p;
    float l = p;
    for (int o = 32; o > 0; o >>= 1) l += __shfl_xor(l, o);
    if ((t & 63) == 0) red[4 + (t >> 6)] = l;
    __syncthreads();
    const u16* vp = vc + ((size_t)h * SK + (SK - W) + c * 128) * DH;
    float o0 = 0.f, o1 = 0.f;
    for (int k = 0; k < 128; ++k) { float v = bf2f(vp[k * DH + t]); o0 += sc[k] * v; o1 += sc[128 + k] * v; }
    float* pp = part + (size_t)b * 2 * 258;
    pp[t] = o0; pp[258 + t] = o1;
    if (t == 0) { pp[256] = fmaxf(red[0], red[1]); pp[257] = red[4] + red[5]; pp[258 + 256] = fmaxf(red[2], red[3]); pp[258 + 257] = red[6] + red[7]; }
}

__global__ void k_comb(const float* part, float* o) {
    int t = threadIdx.x, hq = blockIdx.x, h = hq >> 1, hd = hq & 1;
    float M = -1e30f;
    for (int c = 0; c < 32; ++c) M = fmaxf(M, part[((size_t)(h * 32 + c) * 2 + hd) * 258 + 256]);
    float L = 0.f, acc = 0.f;
    for (int c = 0; c < 32; ++c) {
        const float* pp = part + ((size_t)(h * 32 + c) * 2 + hd) * 258;
        float w = __expf(pp[256] - M);
        L += w * pp[257]; acc += w * pp[t];
    }
    o[hq * 256 + t] = rbf(acc / L);
}

__global__ void k_wo(const float* o, const u16* wo, const u16* xin, u16* xout) {
    __shared__ float red[256];
    int t = threadIdx.x, col = blockIdx.x * 64 + (t & 63), ks = t >> 6;
    float acc = 0.f;
    for (int k = ks * 1024; k < ks * 1024 + 1024; ++k) acc += o[k] * bf2f(wo[(size_t)k * D + col]);
    red[t] = acc;
    __syncthreads();
    if (t < 64) { float a = red[t] + red[t + 64] + red[t + 128] + red[t + 192]; xout[col] = f2bf(bf2f(xin[col]) + rbf(a)); }
}

__global__ void k_mlp1(const u16* x, const u16* w1, const u16* b1, float* hbuf) {
    __shared__ float xs[D], ys[D], zs[D], red[4];
    int t = threadIdx.x;
    for (int i = t; i < D; i += 256) xs[i] = bf2f(x[i]);
    __syncthreads();
    ln_block(xs, ys, red);
    ln_block(ys, zs, red);
    int col = blockIdx.x * 256 + t;
    float acc = 0.f;
    for (int k = 0; k < D; ++k) acc += zs[k] * bf2f(w1[(size_t)k * F + col]);
    float u = rbf(rbf(acc) + bf2f(b1[col]));
    float g = 0.5f * u * (1.f + tanhf(0.7978845608028654f * (u + 0.044715f * u * u * u)));
    hbuf[col] = rbf(g);
}

__global__ void k_mlp2(const float* hbuf, const u16* w2, const u16* b2, const u16* xin, u16* xout) {
    __shared__ float red[256];
    int t = threadIdx.x, col = blockIdx.x * 64 + (t & 63), ks = t >> 6;
    float acc = 0.f;
    for (int k = ks * 448; k < ks * 448 + 448; ++k) acc += hbuf[k] * bf2f(w2[(size_t)k * D + col]);
    red[t] = acc;
    __syncthreads();
    if (t < 64) { float a = red[t] + red[t + 64] + red[t + 128] + red[t + 192]; xout[col] = f2bf(bf2f(xin[col]) + rbf(rbf(a) + bf2f(b2[col]))); }
}

extern "C" void kernel_launch(void* const* d_in, const int* in_sizes, int n_in,
                              void* d_out, int out_size, void* d_ws, size_t ws_size,
                              hipStream_t stream) {
    const u16* x = (const u16*)d_in[0];
    const u16* kc = (const u16*)d_in[1];
    const u16* vc = (const u16*)d_in[2];
    const u16* wq = (const u16*)d_in[3];
    const u16* wo = (const u16*)d_in[4];
    const u16* w1 = (const u16*)d_in[6];
    const u16* b1 = (const u16*)d_in[7];
    const u16* w2 = (const u16*)d_in[8];
    const u16* b2 = (const u16*)d_in[9];
    char* ws = (char*)d_ws;
    float* q = (float*)ws;
    float* o = (float*)(ws + 16384);
    float* hb = (float*)(ws + 32768);
    u16* x1 = (u16*)(ws + 49152);
    u16* x2 = (u16*)(ws + 49152 + 1024);
    u16* x3 = (u16*)(ws + 49152 + 2048);
    float* part = (float*)(ws + 65536);
    const u16* xi = x;
    for (int L = 0; L < 2; ++L) {
        u16* xa = L == 0 ? x1 : x3;
        u16* xb = L == 0 ? x2 : (u16*)d_out;
        k_q<<<16, 256, 0, stream>>>(xi, wq, q);
        k_attn<<<256, 256, 0, stream>>>(q, kc, vc, part);
        k_comb<<<16, 256, 0, stream>>>(part, o);
        k_wo<<<7, 256, 0, stream>>>(o, wo, xi, xa);
        k_mlp1<<<7, 256, 0, stream>>>(xa, w1, b1, hb);
        k_mlp2<<<7, 256, 0, stream>>>(hb, w2, b2, xa, xb);
        xi = xb;
    }
}
```

```cpp
#include <hip/hip_runtime.h>
#include <stdint.h>

typedef unsigned int u32;
typedef unsigned short u16;
typedef unsigned long long u64;
typedef __attribute__((ext_vector_type(4))) u32 u32x4;
typedef __attribute__((ext_vector_type(2))) u32 u32x2;
typedef __attribute__((ext_vector_type(4))) float f32x4;
typedef __attribute__((ext_vector_type(2))) float f32x2;
typedef __attribute__((ext_vector_type(8))) __bf16 bf16x8;
typedef __attribute__((ext_vector_type(2))) __bf16 bf16x2;
typedef __attribute__((ext_vector_type(2))) _Float16 f16x2;
typedef __attribute__((ext_vector_type(2))) unsigned short u16x2;
typedef __amdgpu_buffer_rsrc_t rsrc_t;

namespace mk {

constexpr int SK = 65536, WIN = 4096;
constexpr u32 SENT = 0xFFFFFFFFu;
constexpr int SC1 = 16, NT = 2;
constexpr int SPIN_MAX = 1 << 21;

constexpr int WS_EPOCH = 0;
constexpr int WS_TOK = 4096;
constexpr int WS_Q2 = 32768;
constexpr int WS_X2 = 40960;
constexpr int WS_L = 65536;
constexpr int L_OB = 0, L_ML = 262144, L_C = L_ML + 8192, L_E = L_C + 16384, L_X1 = L_E + 16384, L_SIZE = 327680;
constexpr int WS_TOTAL = WS_L + 2 * L_SIZE;

constexpr int S_Q = 0;
constexpr int S_ZERO = 1280;
constexpr int S_ONES = 1792;
constexpr int S_M = 2048;
constexpr int S_STAT = 2176;
constexpr int S_XA = 2304;
constexpr int S_HA = 3328;
constexpr int S_PART = 4096;
constexpr int S_PART2 = 8192;
constexpr int S_P = 10240;
constexpr int S_TOTAL = 18432;

#define MEMBAR asm volatile("" ::: "memory")
#define BAR() asm volatile("s_waitcnt lgkmcnt(0)\n\ts_barrier" ::: "memory")
#define WAVE_SYNC() do { __builtin_amdgcn_wave_barrier(); MEMBAR; } while (0)

#define STAMP(k)

struct Params {
    const char *x, *kc, *vc, *wq, *wo, *w1, *b1, *w2, *b2;
    char* out;
    char* ws;
};

__device__ __forceinline__ float asf(u32 v) { return __uint_as_float(v); }
__device__ __forceinline__ u32 asu(float v) { return __float_as_uint(v); }
__device__ __forceinline__ u32 pkbf(float a, float b) { return __builtin_bit_cast(u32, __builtin_convertvector((f32x2){a, b}, bf16x2)); }
__device__ __forceinline__ u32 pkf16(float a, float b) { return __builtin_bit_cast(u32, __builtin_convertvector((f32x2){a, b}, f16x2)); }
__device__ __forceinline__ float lo_f(u32 p) { return asf(p << 16); }
__device__ __forceinline__ float hi_f(u32 p) { return asf(p & 0xFFFF0000u); }
__device__ __forceinline__ float rbf(float a) { return lo_f(pkbf(a, a)); }
__device__ __forceinline__ float h_lo(u32 p) { return (float)__builtin_bit_cast(f16x2, p)[0]; }
__device__ __forceinline__ float h_hi(u32 p) { return (float)__builtin_bit_cast(f16x2, p)[1]; }
template <int CTRL> __device__ __forceinline__ u32 dppu(u32 v) { return __builtin_amdgcn_update_dpp(0, v, CTRL, 0xf, 0xf, true); }
template <int CTRL> __device__ __forceinline__ float dppf(float v) { return asf(dppu<CTRL>(asu(v))); }
constexpr int ROR1 = 0x121, ROR2 = 0x122, ROR4 = 0x124, ROR8 = 0x128, QX1 = 0xB1, QX2 = 0x4E;
__device__ __forceinline__ float sum16(float v) { auto s = __builtin_amdgcn_permlane16_swap(asu(v), asu(v), false, false); return asf(s[0]) + asf(s[1]); }
__device__ __forceinline__ float sum32(float v) { auto s = __builtin_amdgcn_permlane32_swap(asu(v), asu(v), false, false); return asf(s[0]) + asf(s[1]); }
__device__ __forceinline__ float max16(float v) { auto s = __builtin_amdgcn_permlane16_swap(asu(v), asu(v), false, false); return fmaxf(asf(s[0]), asf(s[1])); }
__device__ __forceinline__ float max32(float v) { auto s = __builtin_amdgcn_permlane32_swap(asu(v), asu(v), false, false); return fmaxf(asf(s[0]), asf(s[1])); }
__device__ __forceinline__ float tr16(float a, float b) { auto s = __builtin_amdgcn_permlane16_swap(asu(a), asu(b), false, false); return asf(s[0]) + asf(s[1]); }
__device__ __forceinline__ float tr32(float a, float b) { auto s = __builtin_amdgcn_permlane32_swap(asu(a), asu(b), false, false); return asf(s[0]) + asf(s[1]); }
__device__ __forceinline__ float wave_sum(float v) {
    v += dppf<ROR1>(v); v += dppf<ROR2>(v); v += dppf<ROR4>(v); v += dppf<ROR8>(v);
    return sum32(sum16(v));
}
__device__ __forceinline__ rsrc_t mkrs(const void* p, int bytes) { return __builtin_amdgcn_make_buffer_rsrc((void*)p, 0, bytes, 0x00020000); }
#define ld128(r, v, s, aux) __builtin_amdgcn_raw_buffer_load_b128(r, v, s, aux)
__device__ __forceinline__ u32 ld16(rsrc_t r, int v) { return (u32)(u16)__builtin_amdgcn_raw_buffer_load_b16(r, v, 0, 0); }
__device__ __forceinline__ void st128(rsrc_t r, int v, u32x4 d) { __builtin_amdgcn_raw_buffer_store_b128(d, r, v, 0, SC1); }
__device__ __forceinline__ u32 umax(u32 a, u32 b) { return a > b ? a : b; }
__device__ __forceinline__ u32 umin(u32 a, u32 b) { return a < b ? a : b; }
__device__ __forceinline__ bool ok4(u32x4 v) { return umax(umax(v.x, v.y), umax(v.z, v.w)) != SENT; }
__device__ __forceinline__ u32 san(u32 v) { return umin(v, 0xFFFFFFFEu); }
__device__ __forceinline__ f32x4 mfma(u32x4 a, u32x4 b, f32x4 c) {
    return __builtin_amdgcn_mfma_f32_16x16x32_bf16(__builtin_bit_cast(bf16x8, a), __builtin_bit_cast(bf16x8, b), c, 0, 0, 0);
}
__device__ __forceinline__ u32x4 quad_gather(u32 d) { return u32x4{dppu<0x00>(d), dppu<0x55>(d), dppu<0xAA>(d), dppu<0xFF>(d)}; }
__device__ __forceinline__ int colmap(int G, int s) { return 7 * G + ((G & 1) ? (s == 0 ? 6 : s - 1) : s); }
constexpr u32 SEL_LO = 0x05040100u, SEL_HI = 0x07060302u;

template <typename T> __device__ __forceinline__ T& lds(char* sm, int off) { return *(T*)(sm + off); }

template <int N>
__device__ __forceinline__ void gather(rsrc_t rs, const int (&off)[N], u32x4 (&v)[N], bool needx, int xoff, u32& xd) {
    bool okp[N];
#pragma unroll
    for (int i = 0; i < N; ++i) { okp[i] = false; v[i] = u32x4{0, 0, 0, 0}; }
    bool okx = !needx;
    int spins = 0;
    while (true) {
        MEMBAR;
#pragma unroll
        for (int i = 0; i < N; ++i) if (!okp[i]) v[i] = ld128(rs, off[i], 0, SC1);
        if (!okx) xd = __builtin_amdgcn_raw_buffer_load_b32(rs, xoff, 0, SC1);
        bool ok = true;
#pragma unroll
        for (int i = 0; i < N; ++i) { okp[i] = ok4(v[i]); ok = ok & okp[i]; }
        okx = okx | (xd != SENT);
        ok = ok & okx;
        if (__all(ok) || ++spins > SPIN_MAX) break;
    }
}

template <int NR>
__device__ __forceinline__ void prep7(u32x4 (&X)[NR], bool odd) {
    u32x4 B[7];
    const u32 sel0 = odd ? SEL_HI : SEL_LO;
#pragma unroll
    for (int jj = 0; jj < 4; ++jj) {
        u32x4 a = X[2 * jj];
        u32x4 b = (2 * jj + 1 < NR) ? X[(2 * jj + 1 < NR) ? 2 * jj + 1 : 0] : u32x4{0, 0, 0, 0};
#pragma unroll
        for (int e = 1; e < 7; ++e) B[e][jj] = __builtin_amdgcn_perm(b[e >> 1], a[e >> 1], (e & 1) ? SEL_HI : SEL_LO);
        B[0][jj] = __builtin_amdgcn_perm(odd ? b[3] : b[0], odd ? a[3] : a[0], sel0);
    }
#pragma unroll
    for (int e = 0; e < 7; ++e) X[e] = B[e];
}

__device__ __forceinline__ u32x4 make_granule(float a, float b, int pr) {
    float am = fmaxf(fabsf(a), fabsf(b));
    u32 e = asu(am) >> 23;
    e = umax(e, dppu<QX1>(e));
    e = umax(e, dppu<QX2>(e));
    e = umax(e, 15u);
    float inv = asf((268u - e) << 23);
    u32 d = pkf16(a * inv, b * inv);
    d = (pr == 3) ? ((d & 0xFFFFu) | ((e - 14u) << 23)) : san(d);
    return quad_gather(d);
}

__device__ __forceinline__ void a_wave(const Params& P, char* sm, int h, int c, int wv, int l, int nrole) {
    const int r = l & 15, g = l >> 4;
    const size_t kvoff = ((size_t)h * SK + (SK - WIN) + c * 128 + wv * 32) * 512;
    rsrc_t rk = mkrs(P.kc + kvoff, 16384), rv = mkrs(P.vc + kvoff, 16384);
    u32x4 K[16], V[16];
    const int vk = (r >> 1) * 512 + 64 * (r & 1) + 16 * g;
#pragma unroll
    for (int i = 0; i < 16; ++i) K[i] = ld128(rk, vk + 128 * (i & 3), (i >> 2) * 4096, NT);
    const int vv = (g & 1) * 1024 + (g >> 1) * 256 + 16 * r;
#pragma unroll
    for (int i = 0; i < 16; ++i) {
        const int j = i & 7;
        V[i] = ld128(rv, vv + ((j & 1) + 4 * (j >> 2)) * 512, (16 * (i >> 3) + 8 * ((j >> 1) & 1)) * 512, NT);
    }
    STAMP(0);
    BAR();
    const int qa = S_Q + (r & 1) * 640 + ((r >> 2) & 1) * 64 + 16 * g;
    const int qa0 = (r & 2) ? S_ZERO : qa, qa1 = (r & 2) ? qa : S_ZERO;
    const bool half = (r >> 2) & 1;
    const u32 mk0 = ((r >> 2) == 2 * (g >> 1)) ? ~0u : 0u, mk1 = ((r >> 2) == 2 * (g >> 1) + 1) ? ~0u : 0u;
    const float CS = 1.4426950408889634f / 16.0f;
#pragma unroll 1
    for (int L = 0; L < 2; ++L) {
        BAR();
        STAMP(1 + 8 * L);
        f32x4 D0 = {0, 0, 0, 0}, D1 = {0, 0, 0, 0};
#pragma unroll
        for (int s = 0; s < 4; ++s) {
            u32x4 q0 = lds<u32x4>(sm, qa0 + 128 * s);
            D0 = mfma(K[s], q0, D0);
            D1 = mfma(K[4 + s], q0, D1);
        }
#pragma unroll
        for (int s = 0; s < 4; ++s) {
            u32x4 q1 = lds<u32x4>(sm, qa1 + 128 * s);
            D0 = mfma(K[8 + s], q1, D0);
            D1 = mfma(K[12 + s], q1, D1);
        }
        float S0 = half ? D0[1] : D0[0], S1 = half ? D0[3] : D0[2], S2 = half ? D1[1] : D1[0], S3 = half ? D1[3] : D1[2];
        S0 += dppf<ROR4>(S0); S1 += dppf<ROR4>(S1); S2 += dppf<ROR4>(S2); S3 += dppf<ROR4>(S3);
        float m = fmaxf(fmaxf(S0, S1), fmaxf(S2, S3));
        m = fmaxf(m, dppf<QX2>(m));
        m = max32(max16(m));
        const float mc = rbf(m) * CS;
        const u32 r0 = pkbf(S0, S1), r1 = pkbf(S2, S3);
        const float p0 = __builtin_amdgcn_exp2f(fmaf(lo_f(r0), CS, -mc)), p1 = __builtin_amdgcn_exp2f(fmaf(hi_f(r0), CS, -mc));
        const float p2 = __builtin_amdgcn_exp2f(fmaf(lo_f(r1), CS, -mc)), p3 = __builtin_amdgcn_exp2f(fmaf(hi_f(r1), CS, -mc));
        const u32 pk0 = pkbf(p0, p1), pk1 = pkbf(p2, p3);
        auto s0 = __builtin_amdgcn_permlane32_swap(pk0, pk0, false, false);
        auto s1 = __builtin_amdgcn_permlane32_swap(pk1, pk1, false, false);
        const u32x4 A0 = {s0[0] & mk0, s1[0] & mk0, s0[1] & mk0, s1[1] & mk0};
        const u32x4 A1 = {s0[0] & mk1, s1[0] & mk1, s0[1] & mk1, s1[1] & mk1};
        float ls = __builtin_amdgcn_fdot2_f32_bf16(__builtin_bit_cast(bf16x2, pk0), __builtin_bit_cast(bf16x2, 0x3F803F80u), 0.f, false);
        ls = __builtin_amdgcn_fdot2_f32_bf16(__builtin_bit_cast(bf16x2, pk1), __builtin_bit_cast(bf16x2, 0x3F803F80u), ls, false);
        ls += dppf<QX2>(ls);
        ls = sum32(sum16(ls));
        if (l < 2) { lds<float>(sm, S_M + l * 16 + wv * 4) = mc; lds<float>(sm, S_M + 32 + l * 16 + wv * 4) = ls; }
        STAMP(2 + 8 * L);
        f32x4 acc[2][4];
        u16x2 vm = {0, 0};
#pragma unroll
        for (int kg = 0; kg < 2; ++kg) {
            if (L == 0) {
                u32x4 B[8];
#pragma unroll
                for (int jj = 0; jj < 4; ++jj) {
                    const u32x4 a = V[8 * kg + 2 * jj], b = V[8 * kg + 2 * jj + 1];
#pragma unroll
                    for (int e = 0; e < 4; ++e) {
                        B[2 * e][jj] = __builtin_amdgcn_perm(b[e], a[e], SEL_LO);
                        B[2 * e + 1][jj] = __builtin_amdgcn_perm(b[e], a[e], SEL_HI);
                        vm = __builtin_elementwise_max(vm, __builtin_bit_cast(u16x2, a[e] & 0x7FFF7FFFu));
                        vm = __builtin_elementwise_max(vm, __builtin_bit_cast(u16x2, b[e] & 0x7FFF7FFFu));
                    }
                }
#pragma unroll
                for (int d = 0; d < 8; ++d) V[8 * kg + d] = B[d];
            }
#pragma unroll
            for (int a = 0; a < 4; ++a) {
                acc[kg][a] = mfma(A0, V[8 * kg + 2 * a], f32x4{0, 0, 0, 0});
                acc[kg][a] = mfma(A1, V[8 * kg + 2 * a + 1], acc[kg][a]);
            }
        }
        if (L == 0) {
            u32 e = umax((u32)vm[0], (u32)vm[1]) >> 7;
            e = umax(e, dppu<ROR1>(e)); e = umax(e, dppu<ROR2>(e)); e = umax(e, dppu<ROR4>(e)); e = umax(e, dppu<ROR8>(e));
            auto t16 = __builtin_amdgcn_permlane16_swap(e, e, false, false); e = umax(t16[0], t16[1]);
            auto t32 = __builtin_amdgcn_permlane32_swap(e, e, false, false); e = umax(t32[0], t32[1]);
            if (l == 0) lds<u32>(sm, S_M + 64 + wv * 4) = e;
        }
        const int pa = S_P + wv * 2048 + ((g >> 1) * 16 + r) * 32 + (g & 1) * 16;
        lds<f32x4>(sm, pa) = f32x4{acc[0][0][0] + acc[1][0][2], acc[0][1][0] + acc[1][1][2], acc[0][2][0] + acc[1][2][2], acc[0][3][0] + acc[1][3][2]};
        lds<f32x4>(sm, pa + 1024) = f32x4{acc[0][0][1] + acc[1][0][3], acc[0][1][1] + acc[1][1][3], acc[0][2][1] + acc[1][2][3], acc[0][3][1] + acc[1][3][3]};
        STAMP(3 + 8 * L);
        BAR();
        if (L == 0) for (int i = 0; i < nrole; ++i) BAR();
    }
}

__device__ __forceinline__ f32x2 vecsum(rsrc_t rw, int base, int G, int g, bool needx, int xoff, u32& xd) {
    int off[4];
#pragma unroll
    for (int i = 0; i < 4; ++i) off[i] = base + (g + 4 * i) * 1024 + G * 16;
    u32x4 v[4];
    gather<4>(rw, off, v, needx, xoff, xd);
    float a[8];
#pragma unroll
    for (int s = 0; s < 8; ++s) a[s] = 0.f;
#pragma unroll
    for (int i = 0; i < 4; ++i) {
        const float sc = hi_f(v[i].w);
        a[0] = fmaf(h_lo(v[i].x), sc, a[0]); a[1] = fmaf(h_hi(v[i].x), sc, a[1]);
        a[2] = fmaf(h_lo(v[i].y), sc, a[2]); a[3] = fmaf(h_hi(v[i].y), sc, a[3]);
        a[4] = fmaf(h_lo(v[i].z), sc, a[4]); a[5] = fmaf(h_hi(v[i].z), sc, a[5]);
        a[6] = fmaf(h_lo(v[i].w), sc, a[6]);
    }
    const float u0 = tr32(a[0], a[4]), u1 = tr32(a[1], a[5]), u2 = tr32(a[2], a[6]), u3 = tr32(a[3], a[7]);
    return f32x2{tr16(u0, u2), tr16(u1, u3)};
}

__device__ __forceinline__ void wave_stats(char* sm, u32 xp, int g, int rho, int l) {
    const float xa = lo_f(xp), xb = hi_f(xp);
    const float piv = asf(__builtin_amdgcn_readfirstlane(asu(xa)));
    const float da = xa - piv, db = (g == 3) ? 0.f : xb - piv;
    float z = tr32(da + db, fmaf(da, da, db * db));
    z = sum16(z);
    z += dppf<ROR1>(z); z += dppf<ROR2>(z); z += dppf<ROR4>(z); z += dppf<ROR8>(z);
    const float S1 = asf(__builtin_amdgcn_readlane(asu(z), 0)), S2 = asf(__builtin_amdgcn_readlane(asu(z), 32));
    const float t = S1 * (1.0f / 112.0f);
    if (l == 0) { lds<float>(sm, S_STAT + rho * 4) = piv + t; lds<float>(sm, S_STAT + 16 + rho * 4) = fmaf(-t, S1, S2); }
}

template <bool TWICE>
__device__ __forceinline__ void ln_coef(char* sm, float& mu, float& R) {
    const f32x4 mw = lds<f32x4>(sm, S_STAT), m2 = lds<f32x4>(sm, S_STAT + 16);
    const float mean = 0.25f * ((mw[0] + mw[1]) + (mw[2] + mw[3]));
    const float d0 = mw[0] - mean, d1 = mw[1] - mean, d2 = mw[2] - mean, d3 = mw[3] - mean;
    const float dd = fmaf(d0, d0, d1 * d1) + fmaf(d2, d2, d3 * d3);
    const float var = fmaxf(fmaf(dd, 112.0f, (m2[0] + m2[1]) + (m2[2] + m2[3])) * (1.0f / 448.0f), 0.f);
    const float mh = rbf(mean);
    const float dh = rbf(__builtin_amdgcn_sqrtf(rbf(rbf(var) + 1e-5f)));
    const float rd = __builtin_amdgcn_rcpf(dh);
    if (!TWICE) { mu = mh; R = rd; return; }
    const float mh2 = rbf((mean - mh) * rd);
    const float dh2 = rbf(__builtin_amdgcn_sqrtf(rbf(rbf(var * rd * rd) + 1e-5f)));
    mu = fmaf(mh2, dh, mh);
    R = rd * __builtin_amdgcn_rcpf(dh2);
}

template <int ROLE>
__device__ __forceinline__ void r_wave(const Params& P, char* sm, int h, int c, int rho, int l, u32 tok0, u32 tok1) {
    const int n = l & 15, g = l >> 4;
    rsrc_t rw = mkrs(P.ws, WS_TOTAL);
    const int win = 14 * n - 2 * (n & 1);
    const bool odd = n & 1;
    if ((ROLE == 2 || ROLE == 3) && rho == 3) {
        int spins = 0;
        while (true) {
            MEMBAR;
            bool ok = true;
#pragma unroll
            for (int i = 0; i < 4; ++i) {
                u32x2 tk = __builtin_amdgcn_raw_buffer_load_b64(rw, WS_TOK + (l + 64 * i) * 64, 0, SC1);
                ok = ok & (tk.x == tok0) & (tk.y == tok1);
            }
            if (__all(ok) || ++spins > SPIN_MAX) break;
        }
    }
    constexpr int NB8 = 2, NB7 = 6;
    u32x4 W8[NB8][8], W7[NB7][7], WQ[4][4];
    const int G = 16 * rho + n;
    u32 xd = 0, bias = 0;
    int cA = 0, cB = 0;
    const int qh = 2 * h + (c & 1), cb = c >> 1;
    const int sl = 2 * h + ((c - 8) >> 1), hf = (c - 8) & 1;
    const int beta = rho >> 1, kh = rho & 1;
    if (ROLE == 1) {
        rsrc_t r = mkrs(P.wo, 4096 * 896);
        const int v = (qh * 256 + 8 * (8 * rho + g)) * 896 + cb * 224 + win;
#pragma unroll
        for (int k = 0; k < 2; ++k)
#pragma unroll
            for (int j = 0; j < 8; ++j) W8[k][j] = ld128(r, v, (32 * k + 2 * (j & 3) + (j >> 2)) * 896, NT);
    }
    if (ROLE == 2 || ROLE == 3) {
        cA = colmap(G, 2 * g); cB = (g == 3) ? 448 : colmap(G, 2 * g + 1);
    }
    if (ROLE == 2) {
        rsrc_t rx = mkrs(P.x, 896);
        xd = ld16(rx, cA * 2) | (ld16(rx, cB * 2) << 16);
        rsrc_t rb = mkrs(P.b1, 3584);
        bias = ld16(rb, (l & 7) == 7 ? 4096 : (112 * sl + colmap(8 * kh + (l >> 3), l & 7)) * 2);
        rsrc_t r1 = mkrs(P.w1, 448 * 3584);
        const int rb1 = 7 * (16 * rho + g);
        const int v1 = (rb1 - (g & 1)) * 3584 + sl * 224 + win, v0 = (rb1 + 6 * (g & 1)) * 3584 + sl * 224 + win;
#pragma unroll
        for (int k = 0; k < 4; ++k)
#pragma unroll
            for (int j = 0; j < 7; ++j) W7[k][j] = ld128(r1, j ? v1 : v0, (28 * k + j) * 3584, NT);
        rsrc_t r2 = mkrs(P.w2, 1792 * 896);
        const int rb2 = 112 * sl + 7 * (8 * kh + g);
        const int cofs = hf * 448 + beta * 224 + win;
        const int u1 = (rb2 - (g & 1)) * 896 + cofs, u0 = (rb2 + 6 * (g & 1)) * 896 + cofs;
#pragma unroll
        for (int k = 0; k < 2; ++k)
#pragma unroll
            for (int j = 0; j < 7; ++j) W7[NB7 - 2 + k][j] = ld128(r2, j ? u1 : u0, (28 * k + j) * 896, NT);
    }
    if (ROLE == 3) {
        rsrc_t rb = mkrs(P.b2, 896);
        bias = ld16(rb, cA * 2) | (ld16(rb, cB * 2) << 16);
        rsrc_t r = mkrs(P.wq, 448 * 8192);
        const int rb1 = 7 * (16 * rho + g);
        const int cofs = (512 * h + 64 * (c - 12)) * 2 + 8 * n;
        const int v1 = (rb1 - (g & 1)) * 8192 + cofs, v0 = (rb1 + 6 * (g & 1)) * 8192 + cofs;
#pragma unroll
        for (int k = 0; k < 4; ++k)
#pragma unroll
            for (int j = 0; j < 8; ++j) {
                u32x2 w = {0, 0};
                if (j < 7) w = __builtin_amdgcn_raw_buffer_load_b64(r, j ? v1 : v0, (28 * k + j) * 8192, NT);
                WQ[k][j >> 1][2 * (j & 1)] = w.x; WQ[k][j >> 1][2 * (j & 1) + 1] = w.y;
            }
    }
    const int xa_w = S_XA + rho * 256 + n * 16 + g * 4;
    const int xa_r = (n < 8) ? S_XA + rho * 256 + g * 16 : S_ONES;
    STAMP(4);
#pragma unroll 1
    for (int L = 0; L < 2; ++L) {
        const int lb = WS_L + L * L_SIZE;
        BAR();
        STAMP(5 + 8 * L);
        {
            const int hd = rho >> 1, gam = 16 * (rho & 1) + (l >> 2), pr = l & 3;
            const f32x4 m4 = lds<f32x4>(sm, S_M + hd * 16), l4 = lds<f32x4>(sm, S_M + 32 + hd * 16);
            const u32x4 e4 = lds<u32x4>(sm, S_M + 64);
            f32x2 pv[4];
#pragma unroll
            for (int w = 0; w < 4; ++w) pv[w] = lds<f32x2>(sm, S_P + w * 2048 + hd * 1024 + gam * 32 + pr * 8);
            const float M = fmaxf(fmaxf(m4[0], m4[1]), fmaxf(m4[2], m4[3]));
            const u32 ev = umax(umax(umax(e4[0], e4[1]), umax(e4[2], e4[3])), 8u);
            const float sinv = asf((261u - ev) << 23);
            float o0 = 0.f, o1 = 0.f, Ls = 0.f;
#pragma unroll
            for (int w = 0; w < 4; ++w) {
                const float e = __builtin_amdgcn_exp2f(m4[w] - M);
                o0 = fmaf(e, pv[w][0], o0); o1 = fmaf(e, pv[w][1], o1); Ls = fmaf(e, l4[w], Ls);
            }
            const u32x4 gr = quad_gather(san(pkf16(o0 * sinv, o1 * sinv)));
            if (pr == 0) st128(rw, lb + L_OB + (2 * h + hd) * 16384 + c * 512 + gam * 16, gr);
            if (l == 1 && (rho & 1) == 0) st128(rw, lb + L_ML + ((2 * h + hd) * 32 + c) * 16, u32x4{san(asu(M)), san(asu(Ls)), (ev - 7u) << 23, 0u});
        }
        STAMP(6 + 8 * L);
        if (L == 0) {
            if (ROLE == 1) { prep7<8>(W8[0], odd); prep7<8>(W8[1], odd); }
            if (ROLE == 2) {
#pragma unroll
                for (int k = 0; k < NB7; ++k) prep7<7>(W7[k], odd);
            }
            if (ROLE == 3) {
#pragma unroll
                for (int k = 0; k < 4; ++k) {
                    u32x4 B[4];
#pragma unroll
                    for (int jj = 0; jj < 4; ++jj)
#pragma unroll
                        for (int e = 0; e < 4; ++e) B[e][jj] = __builtin_amdgcn_perm(WQ[k][jj][2 + (e >> 1)], WQ[k][jj][e >> 1], (e & 1) ? SEL_HI : SEL_LO);
#pragma unroll
                    for (int e = 0; e < 4; ++e) WQ[k][e] = B[e];
                }
            }
        }
        STAMP(7 + 8 * L);
        if (ROLE == 1) {
            int off[8];
#pragma unroll
            for (int i = 0; i < 4; ++i) {
                off[i] = lb + L_ML + (qh * 32 + 8 * i + (n >> 1)) * 16;
                off[4 + i] = lb + L_OB + qh * 16384 + (8 * i + (n >> 1)) * 512 + rho * 128 + (n & 1) * 64 + g * 16;
            }
            u32x4 v[8];
            gather<8>(rw, off, v, false, 0, xd);
            STAMP(8 + 8 * L);
            float Mx = fmaxf(fmaxf(asf(v[0].x), asf(v[1].x)), fmaxf(asf(v[2].x), asf(v[3].x)));
            Mx = fmaxf(Mx, dppf<ROR2>(Mx)); Mx = fmaxf(Mx, dppf<ROR4>(Mx)); Mx = fmaxf(Mx, dppf<ROR8>(Mx));
            float a[8], Lp = 0.f;
#pragma unroll
            for (int s = 0; s < 8; ++s) a[s] = 0.f;
#pragma unroll
            for (int i = 0; i < 4; ++i) {
                const float e = __builtin_amdgcn_exp2f(asf(v[i].x) - Mx);
                Lp = fmaf(e, asf(v[i].y), Lp);
                const float w = e * asf(v[i].z);
#pragma unroll
                for (int s = 0; s < 4; ++s) { a[2 * s] = fmaf(h_lo(v[4 + i][s]), w, a[2 * s]); a[2 * s + 1] = fmaf(h_hi(v[4 + i][s]), w, a[2 * s + 1]); }
            }
            const u32x4 A = {pkbf(a[0], a[1]), pkbf(a[2], a[3]), pkbf(a[4], a[5]), pkbf(a[6], a[7])};
            float t[8];
#pragma unroll
            for (int e = 0; e < 7; ++e) {
                const f32x4 d0 = mfma(A, W8[0][e], f32x4{0, 0, 0, 0}), d1 = mfma(A, W8[1][e], f32x4{0, 0, 0, 0});
                t[e] = (d0[0] + d0[2]) + (d1[1] + d1[3]);
            }
            t[7] = 0.f;
            Lp += dppf<ROR2>(Lp); Lp += dppf<ROR4>(Lp); Lp += dppf<ROR8>(Lp);
            const float il = __builtin_amdgcn_rcpf(Lp);
            const float u0 = tr32(t[0], t[4]), u1 = tr32(t[1], t[5]), u2 = tr32(t[2], t[6]), u3 = tr32(t[3], t[7]);
            lds<f32x2>(sm, S_PART + rho * 512 + n * 32 + g * 8) = f32x2{tr16(u0, u2) * il, tr16(u1, u3) * il};
            BAR();
            if (rho == 0) {
                f32x2 s = lds<f32x2>(sm, S_PART + l * 8) + lds<f32x2>(sm, S_PART + 512 + l * 8) + (lds<f32x2>(sm, S_PART + 1024 + l * 8) + lds<f32x2>(sm, S_PART + 1536 + l * 8));
                const u32x4 gr = make_granule(s[0], s[1], l & 3);
                if ((l & 3) == 0) st128(rw, lb + L_C + qh * 1024 + (16 * cb + (l >> 2)) * 16, gr);
            }
        }
        if (ROLE == 2) {
            const f32x2 s = vecsum(rw, lb + L_C, G, g, L == 1, WS_X2 + G * 16 + g * 4, xd);
            STAMP(8 + 8 * L);
            const u32 sb = pkbf(s[0], s[1]);
            const u32 xp = pkbf(lo_f(xd) + lo_f(sb), hi_f(xd) + hi_f(sb));
            lds<u32>(sm, xa_w) = xp;
            WAVE_SYNC();
            f32x4 acc[7];
#pragma unroll
            for (int e = 0; e < 7; ++e) acc[e] = f32x4{0, 0, 0, 0};
#pragma unroll
            for (int k = 0; k < 4; ++k) {
                const u32x4 A = lds<u32x4>(sm, xa_r + 64 * k);
#pragma unroll
                for (int e = 0; e < 7; ++e) acc[e] = mfma(A, W7[k][e], acc[e]);
            }
            wave_stats(sm, xp, g, rho, l);
            if (c == 8 && h == 0 && l < 16) {
                u32x4 xg = lds<u32x4>(sm, S_XA + rho * 256 + l * 16);
                st128(rw, lb + L_X1 + (16 * rho + l) * 16, u32x4{san(xg.x), san(xg.y), san(xg.z), san(xg.w)});
            }
            lds<f32x4>(sm, S_PART + rho * 1024 + (g >> 1) * 512 + n * 32 + (g & 1) * 16) =
                (g & 1) ? f32x4{acc[4][0], acc[5][0], acc[6][0], 0.f} : f32x4{acc[0][0], acc[1][0], acc[2][0], acc[3][0]};
            BAR();
            STAMP(9 + 8 * L);
            {
                const int pa = S_PART + (64 * kh + l) * 4;
                const float ps = (lds<float>(sm, pa) + lds<float>(sm, pa + 1024)) + (lds<float>(sm, pa + 2048) + lds<float>(sm, pa + 3072));
                const float ss = (lds<float>(sm, pa + 512) + lds<float>(sm, pa + 1536)) + (lds<float>(sm, pa + 2560) + lds<float>(sm, pa + 3584));
                float mu, R;
                ln_coef<true>(sm, mu, R);
                const float u = rbf(rbf(R * fmaf(-mu, ss, ps)) + lo_f(bias));
                const float y = u * fmaf(u * u, 0.044715f, 1.0f) * (-2.0f * 0.7978845608028654f * 1.4426950408889634f);
                const float hv = u * __builtin_amdgcn_rcpf(1.0f + __builtin_amdgcn_exp2f(y));
                lds<u16>(sm, S_HA + rho * 128 + l * 2) = ((l & 7) == 7) ? (u16)0 : (u16)pkbf(hv, hv);
            }
            WAVE_SYNC();
#pragma unroll
            for (int e = 0; e < 7; ++e) acc[e] = f32x4{0, 0, 0, 0};
#pragma unroll
            for (int k = 0; k < 2; ++k) {
                const u32x4 A = lds<u32x4>(sm, S_HA + rho * 128 + 64 * k + g * 16);
#pragma unroll
                for (int e = 0; e < 7; ++e) acc[e] = mfma(A, W7[NB7 - 2 + k][e], acc[e]);
            }
            if (g < 2)
                lds<f32x4>(sm, S_PART2 + rho * 512 + n * 32 + (g & 1) * 16) =
                    (g & 1) ? f32x4{acc[4][0], acc[5][0], acc[6][0], 0.f} : f32x4{acc[0][0], acc[1][0], acc[2][0], acc[3][0]};
            BAR();
            if (rho < 2) {
                f32x2 s2 = lds<f32x2>(sm, S_PART2 + rho * 1024 + l * 8) + lds<f32x2>(sm, S_PART2 + rho * 1024 + 512 + l * 8);
                const u32x4 gr = make_granule(s2[0], s2[1], l & 3);
                if ((l & 3) == 0) st128(rw, lb + L_E + sl * 1024 + (32 * hf + 16 * rho + (l >> 2)) * 16, gr);
            }
        }
        if (ROLE == 3) {
            if (L == 1 && !(c == 12 && h == 0)) break;
            const f32x2 s = vecsum(rw, lb + L_E, G, g, true, lb + L_X1 + G * 16 + g * 4, xd);
            STAMP(8 + 8 * L);
            const u32 sb = pkbf(s[0], s[1]);
            const u32 sc = pkbf(lo_f(sb) + lo_f(bias), hi_f(sb) + hi_f(bias));
            const u32 xp = pkbf(lo_f(xd) + lo_f(sc), hi_f(xd) + hi_f(sc));
            if (L == 1) {
                u16* o = (u16*)P.out;
                o[cA] = (u16)xp;
                if (g != 3) o[cB] = (u16)(xp >> 16);
                STAMP(10 + 8 * L);
                break;
            }
            lds<u32>(sm, xa_w) = xp;
            WAVE_SYNC();
            f32x4 acc[4];
#pragma unroll
            for (int e = 0; e < 4; ++e) acc[e] = f32x4{0, 0, 0, 0};
#pragma unroll
            for (int k = 0; k < 4; ++k) {
                const u32x4 A = lds<u32x4>(sm, xa_r + 64 * k);
#pragma unroll
                for (int e = 0; e < 4; ++e) acc[e] = mfma(A, WQ[k][e], acc[e]);
            }
            wave_stats(sm, xp, g, rho, l);
            if (c == 12 && h == 0 && l < 16) {
                u32x4 xg = lds<u32x4>(sm, S_XA + rho * 256 + l * 16);
                st128(rw, WS_X2 + (16 * rho + l) * 16, u32x4{san(xg.x), san(xg.y), san(xg.z), san(xg.w)});
            }
            if ((g & 1) == 0) lds<f32x4>(sm, S_PART + rho * 512 + (g >> 1) * 256 + n * 16) = f32x4{acc[0][0], acc[1][0], acc[2][0], acc[3][0]};
            BAR();
            if (rho == 0) {
                const int pa = S_PART + (l & 31) * 8;
                const f32x2 ps = (lds<f32x2>(sm, pa) + lds<f32x2>(sm, pa + 512)) + (lds<f32x2>(sm, pa + 1024) + lds<f32x2>(sm, pa + 1536));
                const f32x2 ss = (lds<f32x2>(sm, pa + 256) + lds<f32x2>(sm, pa + 768)) + (lds<f32x2>(sm, pa + 1280) + lds<f32x2>(sm, pa + 1792));
                float mu, R;
                ln_coef<false>(sm, mu, R);
                const u32x4 gr = quad_gather(san(pkbf(R * fmaf(-mu, ss[0], ps[0]), R * fmaf(-mu, ss[1], ps[1]))));
                if ((l & 3) == 0 && l < 32) st128(rw, WS_Q2 + (512 * h + 64 * (c - 12)) * 2 + (l >> 2) * 16, gr);
            }
        }
        STAMP(10 + 8 * L);
        if (L == 1) break;
        if (rho == 3) {
            int off[1] = {WS_Q2 + h * 1024 + l * 16};
            u32x4 v[1];
            gather<1>(rw, off, v, false, 0, xd);
            lds<u32x4>(sm, S_Q + (l >> 5) * 640 + (l & 31) * 16) = v[0];
            if (h == 0 && c == 0 && l == 0) st128(rw, WS_EPOCH, u32x4{tok0, tok1, 0u, 0u});
        }
        STAMP(11);
        BAR();
    }
}

__global__ __launch_bounds__(512, 2) void mega(Params P) {
    __shared__ __attribute__((aligned(16))) char sm[S_TOTAL];
    const int t = threadIdx.x, l = t & 63, wv = __builtin_amdgcn_readfirstlane(t >> 6);
    const int b = blockIdx.x, h = b & 7, c = b >> 3;
    const int role = c < 8 ? 1 : c < 12 ? 2 : c < 20 ? 3 : 0;
    if (wv < 4) {
        a_wave(P, sm, h, c, wv, l, role == 2 ? 2 : role == 0 ? 0 : 1);
        return;
    }
    const int rho = wv - 4, tt = t - 256;
    rsrc_t rw = mkrs(P.ws, WS_TOTAL);
    STAMP(0);
    rsrc_t rx = mkrs(P.x, 896), rq = mkrs(P.wq, 448 * 8192);
    float xs[7], xr[4];
    u32x4 wr[4];
#pragma unroll
    for (int i = 0; i < 7; ++i) xs[i] = lo_f(ld16(rx, (l + 64 * i) * 2));
#pragma unroll
    for (int i = 0; i < 4; ++i) {
        const int row = (tt >> 1) + 128 * i;
        wr[i] = ld128(rq, row * 8192 + (512 * h + 16 * c + 8 * (tt & 1)) * 2, 0, 0);
        xr[i] = lo_f(ld16(rx, row * 2));
    }
    u32 tok0 = 0, tok1 = 0;
    if (rho == 3) {
        const u32x2 ep = __builtin_amdgcn_raw_buffer_load_b64(rw, WS_EPOCH, 0, SC1);
        const u64 tk = (((u64)ep.y << 32) | ep.x) + 1;
        tok0 = (u32)tk; tok1 = (u32)(tk >> 32);
        const u32x4 S4 = {SENT, SENT, SENT, SENT};
        for (int L = 0; L < 2; ++L) st128(rw, WS_L + L * L_SIZE + L_OB + (2 * h + (l >> 5)) * 16384 + c * 512 + (l & 31) * 16, S4);
        if (l < 4) st128(rw, WS_L + (l >> 1) * L_SIZE + L_ML + ((2 * h + (l & 1)) * 32 + c) * 16, S4);
        if (role == 1 && l < 32) st128(rw, WS_L + (l >> 4) * L_SIZE + L_C + (2 * h + (c & 1)) * 1024 + (16 * (c >> 1) + (l & 15)) * 16, S4);
        if (role == 2) {
            st128(rw, WS_L + (l >> 5) * L_SIZE + L_E + (2 * h + ((c - 8) >> 1)) * 1024 + (32 * ((c - 8) & 1) + (l & 31)) * 16, S4);
            if (c == 8 && h == 0) for (int L = 0; L < 2; ++L) st128(rw, WS_L + L * L_SIZE + L_X1 + l * 16, S4);
        }
        if (role == 3) {
            if (l < 8) st128(rw, WS_Q2 + (512 * h + 64 * (c - 12)) * 2 + l * 16, S4);
            if (c == 12 && h == 0) st128(rw, WS_X2 + l * 16, S4);
        }
    }
    if (rho == 0) {
        if (l < 32) lds<u32x4>(sm, S_ZERO + l * 16) = u32x4{0, 0, 0, 0};
        else if (l < 48) lds<u32x4>(sm, S_ONES + (l - 32) * 16) = u32x4{0x3F803F80u, 0x3F803F80u, 0x3F803F80u, 0x3F803F80u};
    }
    {
        float s = 0.f;
#pragma unroll
        for (int i = 0; i < 7; ++i) s += xs[i];
        const float mean = wave_sum(s) * (1.0f / 448.0f);
        float v = 0.f;
#pragma unroll
        for (int i = 0; i < 7; ++i) { const float d = xs[i] - mean; v = fmaf(d, d, v); }
        const float var = wave_sum(v) * (1.0f / 448.0f);
        const float mh = rbf(mean), dh = rbf(sqrtf(rbf(rbf(var) + 1e-5f)));
        float a[8];
#pragma unroll
        for (int k = 0; k < 8; ++k) a[k] = 0.f;
#pragma unroll
        for (int i = 0; i < 4; ++i) {
            const float y = rbf(rbf(xr[i] - mh) / dh);
#pragma unroll
            for (int k = 0; k < 4; ++k) { a[2 * k] = fmaf(y, lo_f(wr[i][k]), a[2 * k]); a[2 * k + 1] = fmaf(y, hi_f(wr[i][k]), a[2 * k + 1]); }
        }
        lds<f32x4>(sm, S_P + tt * 32) = f32x4{a[0], a[1], a[2], a[3]};
        lds<f32x4>(sm, S_P + tt * 32 + 16) = f32x4{a[4], a[5], a[6], a[7]};
    }
    STAMP(1);
    BAR();
    if (rho == 3) {
        float s = 0.f;
#pragma unroll
        for (int k = 0; k < 32; ++k) s += lds<float>(sm, S_P + ((l >> 4) * 32 + k) * 64 + (l & 15) * 4);
        s = sum32(sum16(s));
        const u32 d0 = pkbf(s, dppf<0x55>(s)), d1 = pkbf(dppf<0xAA>(s), dppf<0xFF>(s));
        asm volatile("s_waitcnt vmcnt(0)" ::: "memory");
        if (l < 16 && (l & 3) == 0) st128(rw, WS_TOK + (h * 32 + c) * 64 + (l >> 2) * 16, u32x4{tok0, tok1, d0, d1});
        STAMP(2);
        u32x4 v0, v1;
        int spins = 0;
        while (true) {
            MEMBAR;
            v0 = ld128(rw, WS_TOK + h * 2048 + l * 16, 0, SC1);
            v1 = ld128(rw, WS_TOK + h * 2048 + 1024 + l * 16, 0, SC1);
            const bool ok = (v0.x == tok0) & (v0.y == tok1) & (v1.x == tok0) & (v1.y == tok1);
            if (__all(ok) || ++spins > SPIN_MAX) break;
        }
        lds<u32x2>(sm, S_Q + l * 8) = u32x2{v0.z, v0.w};
        lds<u32x2>(sm, S_Q + 640 + l * 8) = u32x2{v1.z, v1.w};
    }
    STAMP(3);
    BAR();
    if (role == 1) r_wave<1>(P, sm, h, c, rho, l, tok0, tok1);
    else if (role == 2) r_wave<2>(P, sm, h, c, rho, l, tok0, tok1);
    else if (role == 3) r_wave<3>(P, sm, h, c, rho, l, tok0, tok1);
    else r_wave<0>(P, sm, h, c, rho, l, tok0, tok1);
}

}

extern "C" void kernel_launch(void* const* d_in, const int* in_sizes, int n_in, void* d_out, int out_size, void* d_ws,
                              size_t ws_size, hipStream_t stream) {
    mk::Params P;
    P.x = (const char*)d_in[0]; P.kc = (const char*)d_in[1]; P.vc = (const char*)d_in[2];
    P.wq = (const char*)d_in[3]; P.wo = (const char*)d_in[4]; P.w1 = (const char*)d_in[6];
    P.b1 = (const char*)d_in[7]; P.w2 = (const char*)d_in[8]; P.b2 = (const char*)d_in[9];
    P.out = (char*)d_out; P.ws = (char*)d_ws;
    mk::mega<<<256, 512, 0, stream>>>(P);
}
```

```cpp
#include <hip/hip_runtime.h>
#include <stdint.h>

typedef unsigned int u32;
typedef unsigned short u16;
typedef unsigned long long u64;
typedef __attribute__((ext_vector_type(4))) u32 u32x4;
typedef __attribute__((ext_vector_type(2))) u32 u32x2;
typedef __attribute__((ext_vector_type(4))) float f32x4;
typedef __attribute__((ext_vector_type(2))) float f32x2;
typedef __attribute__((ext_vector_type(8))) __bf16 bf16x8;
typedef __attribute__((ext_vector_type(2))) __bf16 bf16x2;
typedef __attribute__((ext_vector_type(2))) _Float16 f16x2;
typedef __attribute__((ext_vector_type(2))) unsigned short u16x2;
typedef __amdgpu_buffer_rsrc_t rsrc_t;

namespace mk {

constexpr int SK = 65536, WIN = 4096;
constexpr u32 SENT = 0xFFFFFFFFu;
constexpr int SC1 = 16, NT = 2;
#define SPINLOG 21
constexpr int SPIN_MAX = 1 << SPINLOG;
#define NPOLL 0
#define PDEL0 30
#define PDEL1 55
#define CLRPRE 3
#define QSLEEP 35
#define CLRSLEEP 16

constexpr int WS_EPOCH = 0;
constexpr int WS_CLR = 1024;
constexpr int WS_TOK = 4096;
constexpr int WS_Q2 = 32768;
constexpr int WS_X2 = 40960;
constexpr int WS_L = 65536;
constexpr int L_OB = 0, L_ML = 262144, L_C = L_ML + 8192, L_E = L_C + 16384, L_X1 = L_E + 16384, L_SIZE = 327680;
constexpr int WS_TOTAL = WS_L + 2 * L_SIZE;

constexpr int S_Q = 0;
constexpr int S_ZERO = 1280;
constexpr int S_ONES = 1792;
constexpr int S_M = 2048;
constexpr int S_STAT = 2176;
constexpr int S_FLAG = 2240;
constexpr int S_XA = 2304;
constexpr int S_HA = 3328;
constexpr int S_PART = 4096;
constexpr int S_PART2 = 8192;
constexpr int S_P = 10240;
constexpr int S_TOTAL = 18432;

#define WND 6
#define HOLD 24
#define APF 6944
#define CPF 189
#define ADELAY 2
#define STR_(x) #x
#define STR(x) STR_(x)
#define THROTTLE() asm volatile("s_waitcnt vmcnt(%0)" :: "n"(WND - 1) : "memory")
#define B2 16
#define XFIRST 2
#define QWD 50
#define OWT 0
#define PFE 0
#define MWD 0
#define TGP 0
#define TGO 0
#define TGM 0
#define TGQ 0
#define TGATE(T) do { if (T) { while ((u32)__builtin_amdgcn_s_memrealtime() - t0 < (u32)(T)) __builtin_amdgcn_s_sleep(2); MEMBAR; __builtin_amdgcn_sched_barrier(0); } } while (0)
#define B1 4
#define WAITFLAG(off) do { while (true) { MEMBAR; if (__builtin_amdgcn_readfirstlane(lds<u32>(sm, off)) != 0) break; __builtin_amdgcn_s_sleep(1); } MEMBAR; } while (0)
#define MEMBAR asm volatile("" ::: "memory")
#define BAR() do { __builtin_amdgcn_sched_barrier(0); asm volatile("s_waitcnt lgkmcnt(0)\n\ts_barrier" ::: "memory"); __builtin_amdgcn_sched_barrier(0); } while (0)
#define WAVE_SYNC() do { __builtin_amdgcn_wave_barrier(); MEMBAR; } while (0)

#define STAMP(k)
#define STAMP_DUMP()
#define STAMP_LDS 0

struct Params {
    const char *x, *kc, *vc, *wq, *wo, *w1, *b1, *w2, *b2;
    char* out;
    char* ws;
};

__device__ __forceinline__ float asf(u32 v) { return __uint_as_float(v); }
__device__ __forceinline__ u32 asu(float v) { return __float_as_uint(v); }
__device__ __forceinline__ u32 pkbf(float a, float b) { return __builtin_bit_cast(u32, __builtin_convertvector((f32x2){a, b}, bf16x2)); }
__device__ __forceinline__ u32 pkf16(float a, float b) { return __builtin_bit_cast(u32, __builtin_convertvector((f32x2){a, b}, f16x2)); }
__device__ __forceinline__ float lo_f(u32 p) { return asf(p << 16); }
__device__ __forceinline__ float hi_f(u32 p) { return asf(p & 0xFFFF0000u); }
__device__ __forceinline__ float rbf(float a) { return lo_f(pkbf(a, a)); }
__device__ __forceinline__ float h_lo(u32 p) { return (float)__builtin_bit_cast(f16x2, p)[0]; }
__device__ __forceinline__ float h_hi(u32 p) { return (float)__builtin_bit_cast(f16x2, p)[1]; }
template <int CTRL> __device__ __forceinline__ u32 dppu(u32 v) { return __builtin_amdgcn_update_dpp(0, v, CTRL, 0xf, 0xf, true); }
template <int CTRL> __device__ __forceinline__ float dppf(float v) { return asf(dppu<CTRL>(asu(v))); }
constexpr int ROR1 = 0x121, ROR2 = 0x122, ROR4 = 0x124, ROR8 = 0x128, QX1 = 0xB1, QX2 = 0x4E;
__device__ __forceinline__ float sum16(float v) { auto s = __builtin_amdgcn_permlane16_swap(asu(v), asu(v), false, false); return asf(s[0]) + asf(s[1]); }
__device__ __forceinline__ float sum32(float v) { auto s = __builtin_amdgcn_permlane32_swap(asu(v), asu(v), false, false); return asf(s[0]) + asf(s[1]); }
__device__ __forceinline__ float max16(float v) { auto s = __builtin_amdgcn_permlane16_swap(asu(v), asu(v), false, false); return fmaxf(asf(s[0]), asf(s[1])); }
__device__ __forceinline__ float max32(float v) { auto s = __builtin_amdgcn_permlane32_swap(asu(v), asu(v), false, false); return fmaxf(asf(s[0]), asf(s[1])); }
__device__ __forceinline__ float tr16(float a, float b) { auto s = __builtin_amdgcn_permlane16_swap(asu(a), asu(b), false, false); return asf(s[0]) + asf(s[1]); }
__device__ __forceinline__ float tr32(float a, float b) { auto s = __builtin_amdgcn_permlane32_swap(asu(a), asu(b), false, false); return asf(s[0]) + asf(s[1]); }
__device__ __forceinline__ float wave_sum(float v) {
    v += dppf<ROR1>(v); v += dppf<ROR2>(v); v += dppf<ROR4>(v); v += dppf<ROR8>(v);
    return sum32(sum16(v));
}
__device__ __forceinline__ rsrc_t mkrs(const void* p, int bytes) { return __builtin_amdgcn_make_buffer_rsrc((void*)p, 0, bytes, 0x00020000); }
#define ld128(r, v, s, aux) __builtin_amdgcn_raw_buffer_load_b128(r, v, s, aux)
__device__ __forceinline__ u32 ld16(rsrc_t r, int v) { return (u32)(u16)__builtin_amdgcn_raw_buffer_load_b16(r, v, 0, 0); }
__device__ __forceinline__ void st128(rsrc_t r, int v, u32x4 d) { __builtin_amdgcn_raw_buffer_store_b128(d, r, v, 0, SC1); }
__device__ __forceinline__ u32 umax(u32 a, u32 b) { return a > b ? a : b; }
__device__ __forceinline__ u32 umin(u32 a, u32 b) { return a < b ? a : b; }
__device__ __forceinline__ bool ok4(u32x4 v) { return umax(umax(v.x, v.y), umax(v.z, v.w)) != SENT; }
__device__ __forceinline__ u32 san(u32 v) { return umin(v, 0xFFFFFFFEu); }
__device__ __forceinline__ f32x4 mfma(u32x4 a, u32x4 b, f32x4 c) {
    return __builtin_amdgcn_mfma_f32_16x16x32_bf16(__builtin_bit_cast(bf16x8, a), __builtin_bit_cast(bf16x8, b), c, 0, 0, 0);
}
__device__ __forceinline__ u32x4 quad_gather(u32 d) { return u32x4{dppu<0x00>(d), dppu<0x55>(d), dppu<0xAA>(d), dppu<0xFF>(d)}; }
__device__ __forceinline__ int colmap(int G, int s) { return 7 * G + ((G & 1) ? (s == 0 ? 6 : s - 1) : s); }
constexpr u32 SEL_LO = 0x05040100u, SEL_HI = 0x07060302u;

template <typename T> __device__ __forceinline__ T& lds(char* sm, int off) { return *(T*)(sm + off); }

template <int N>
__device__ __forceinline__ void gather(rsrc_t rs, const int (&off)[N], u32x4 (&v)[N], bool needx, int xoff, u32& xd) {
    bool okp[N];
#pragma unroll
    for (int i = 0; i < N; ++i) { okp[i] = false; v[i] = u32x4{0, 0, 0, 0}; }
    bool okx = !needx;
    int spins = 0;
    while (true) {
        MEMBAR;
#pragma unroll
        for (int i = 0; i < N; ++i) if (!okp[i]) v[i] = ld128(rs, off[i], 0, SC1);
        if (!okx) xd = __builtin_amdgcn_raw_buffer_load_b32(rs, xoff, 0, SC1);
        bool ok = true;
#pragma unroll
        for (int i = 0; i < N; ++i) { okp[i] = ok4(v[i]); ok = ok & okp[i]; }
        okx = okx | (xd != SENT);
        ok = ok & okx;
        if (__all(ok) || ++spins > SPIN_MAX) break;
    }
}

template <int NR>
__device__ __forceinline__ void prep7(u32x4 (&X)[NR], bool odd) {
    u32x4 B[7];
    const u32 sel0 = odd ? SEL_HI : SEL_LO;
#pragma unroll
    for (int jj = 0; jj < 4; ++jj) {
        u32x4 a = X[2 * jj];
        u32x4 b = (2 * jj + 1 < NR) ? X[(2 * jj + 1 < NR) ? 2 * jj + 1 : 0] : u32x4{0, 0, 0, 0};
#pragma unroll
        for (int e = 1; e < 7; ++e) B[e][jj] = __builtin_amdgcn_perm(b[e >> 1], a[e >> 1], (e & 1) ? SEL_HI : SEL_LO);
        B[0][jj] = __builtin_amdgcn_perm(odd ? b[3] : b[0], odd ? a[3] : a[0], sel0);
    }
#pragma unroll
    for (int e = 0; e < 7; ++e) X[e] = B[e];
}

__device__ __forceinline__ u32x4 make_granule(float a, float b, int pr) {
    float am = fmaxf(fabsf(a), fabsf(b));
    u32 e = asu(am) >> 23;
    e = umax(e, dppu<QX1>(e));
    e = umax(e, dppu<QX2>(e));
    e = umax(e, 15u);
    float inv = asf((268u - e) << 23);
    u32 d = pkf16(a * inv, b * inv);
    d = (pr == 3) ? ((d & 0xFFFFu) | ((e - 14u) << 23)) : san(d);
    return quad_gather(d);
}

__device__ __forceinline__ void a_wave(const Params& P, char* sm, int h, int c, int wv, int l, int nrole) {
    const int r = l & 15, g = l >> 4;
    const size_t kvoff = ((size_t)h * SK + (SK - WIN) + c * 128 + wv * 32) * 512;
    rsrc_t rk = mkrs(P.kc + kvoff, 16384), rv = mkrs(P.vc + kvoff, 16384);
    u32x4 K[16], V[16];
    STAMP(18);
    BAR();
    STAMP(19);
    const int vk = (r >> 1) * 512 + 64 * (r & 1) + 16 * g;
    const int vv = (g & 1) * 1024 + (g >> 1) * 256 + 16 * r;
#pragma unroll
    for (int i = 0; i < 32; ++i) {
        if (i == B1) WAITFLAG(S_FLAG + 8);
        if (i == B2) WAITFLAG(S_FLAG + 12);
        if (i < 16) K[i] = ld128(rk, vk + 128 * (i & 3), (i >> 2) * 4096, NT);
        else {
            const int j = i & 7;
            V[i - 16] = ld128(rv, vv + ((j & 1) + 4 * (j >> 2)) * 512, (16 * ((i - 16) >> 3) + 8 * ((j >> 1) & 1)) * 512, NT);
        }
    }
    if (OWT || PFE) { MEMBAR; if (l == 0) __hip_atomic_fetch_add((u32*)(sm + S_FLAG + 16), 1u, __ATOMIC_RELAXED, __HIP_MEMORY_SCOPE_WORKGROUP); MEMBAR; __builtin_amdgcn_sched_barrier(0); }
    STAMP(0);
    const int qa = S_Q + (r & 1) * 640 + ((r >> 2) & 1) * 64 + 16 * g;
    const int qa0 = (r & 2) ? S_ZERO : qa, qa1 = (r & 2) ? qa : S_ZERO;
    const bool half = (r >> 2) & 1;
    const u32 mk0 = ((r >> 2) == 2 * (g >> 1)) ? ~0u : 0u, mk1 = ((r >> 2) == 2 * (g >> 1) + 1) ? ~0u : 0u;
    const float CS = 1.4426950408889634f / 16.0f;
#pragma unroll 1
    for (int L = 0; L < 2; ++L) {
        if (L == 0) {
            while (true) {
                MEMBAR;
                if (__builtin_amdgcn_readfirstlane(lds<u32>(sm, S_FLAG)) != 0) break;
                __builtin_amdgcn_s_sleep(1);
            }
            MEMBAR;
        } else BAR();
        STAMP(1 + 8 * L);
        f32x4 D0 = {0, 0, 0, 0}, D1 = {0, 0, 0, 0};
#pragma unroll
        for (int s = 0; s < 4; ++s) {
            u32x4 q0 = lds<u32x4>(sm, qa0 + 128 * s);
            D0 = mfma(K[s], q0, D0);
            D1 = mfma(K[4 + s], q0, D1);
        }
#pragma unroll
        for (int s = 0; s < 4; ++s) {
            u32x4 q1 = lds<u32x4>(sm, qa1 + 128 * s);
            D0 = mfma(K[8 + s], q1, D0);
            D1 = mfma(K[12 + s], q1, D1);
        }
        float S0 = half ? D0[1] : D0[0], S1 = half ? D0[3] : D0[2], S2 = half ? D1[1] : D1[0], S3 = half ? D1[3] : D1[2];
        S0 += dppf<ROR4>(S0); S1 += dppf<ROR4>(S1); S2 += dppf<ROR4>(S2); S3 += dppf<ROR4>(S3);
        float m = fmaxf(fmaxf(S0, S1), fmaxf(S2, S3));
        m = fmaxf(m, dppf<QX2>(m));
        m = max32(max16(m));
        const float mc = rbf(m) * CS;
        const u32 r0 = pkbf(S0, S1), r1 = pkbf(S2, S3);
        const float p0 = __builtin_amdgcn_exp2f(fmaf(lo_f(r0), CS, -mc)), p1 = __builtin_amdgcn_exp2f(fmaf(hi_f(r0), CS, -mc));
        const float p2 = __builtin_amdgcn_exp2f(fmaf(lo_f(r1), CS, -mc)), p3 = __builtin_amdgcn_exp2f(fmaf(hi_f(r1), CS, -mc));
        const u32 pk0 = pkbf(p0, p1), pk1 = pkbf(p2, p3);
        auto s0 = __builtin_amdgcn_permlane32_swap(pk0, pk0, false, false);
        auto s1 = __builtin_amdgcn_permlane32_swap(pk1, pk1, false, false);
        const u32x4 A0 = {s0[0] & mk0, s1[0] & mk0, s0[1] & mk0, s1[1] & mk0};
        const u32x4 A1 = {s0[0] & mk1, s1[0] & mk1, s0[1] & mk1, s1[1] & mk1};
        float ls = __builtin_amdgcn_fdot2_f32_bf16(__builtin_bit_cast(bf16x2, pk0), __builtin_bit_cast(bf16x2, 0x3F803F80u), 0.f, false);
        ls = __builtin_amdgcn_fdot2_f32_bf16(__builtin_bit_cast(bf16x2, pk1), __builtin_bit_cast(bf16x2, 0x3F803F80u), ls, false);
        ls += dppf<QX2>(ls);
        ls = sum32(sum16(ls));
        if (l < 2) { lds<float>(sm, S_M + l * 16 + wv * 4) = mc; lds<float>(sm, S_M + 32 + l * 16 + wv * 4) = ls; }
        STAMP(2 + 8 * L);
        f32x4 acc[2][4];
        u16x2 vm = {0, 0};
#pragma unroll
        for (int kg = 0; kg < 2; ++kg) {
            if (L == 0) {
                u32x4 B[8];
#pragma unroll
                for (int jj = 0; jj < 4; ++jj) {
                    const u32x4 a = V[8 * kg + 2 * jj], b = V[8 * kg + 2 * jj + 1];
#pragma unroll
                    for (int e = 0; e < 4; ++e) {
                        B[2 * e][jj] = __builtin_amdgcn_perm(b[e], a[e], SEL_LO);
                        B[2 * e + 1][jj] = __builtin_amdgcn_perm(b[e], a[e], SEL_HI);
                        vm = __builtin_elementwise_max(vm, __builtin_bit_cast(u16x2, a[e] & 0x7FFF7FFFu));
                        vm = __builtin_elementwise_max(vm, __builtin_bit_cast(u16x2, b[e] & 0x7FFF7FFFu));
                    }
                }
#pragma unroll
                for (int d = 0; d < 8; ++d) V[8 * kg + d] = B[d];
            }
#pragma unroll
            for (int a = 0; a < 4; ++a) {
                acc[kg][a] = mfma(A0, V[8 * kg + 2 * a], f32x4{0, 0, 0, 0});
                acc[kg][a] = mfma(A1, V[8 * kg + 2 * a + 1], acc[kg][a]);
            }
        }
        if (L == 0) {
            u32 e = umax((u32)vm[0], (u32)vm[1]) >> 7;
            e = umax(e, dppu<ROR1>(e)); e = umax(e, dppu<ROR2>(e)); e = umax(e, dppu<ROR4>(e)); e = umax(e, dppu<ROR8>(e));
            auto t16 = __builtin_amdgcn_permlane16_swap(e, e, false, false); e = umax(t16[0], t16[1]);
            auto t32 = __builtin_amdgcn_permlane32_swap(e, e, false, false); e = umax(t32[0], t32[1]);
            if (l == 0) lds<u32>(sm, S_M + 64 + wv * 4) = e;
        }
        const int pa = S_P + wv * 2048 + ((g >> 1) * 16 + r) * 32 + (g & 1) * 16;
        lds<f32x4>(sm, pa) = f32x4{acc[0][0][0] + acc[1][0][2], acc[0][1][0] + acc[1][1][2], acc[0][2][0] + acc[1][2][2], acc[0][3][0] + acc[1][3][2]};
        lds<f32x4>(sm, pa + 1024) = f32x4{acc[0][0][1] + acc[1][0][3], acc[0][1][1] + acc[1][1][3], acc[0][2][1] + acc[1][2][3], acc[0][3][1] + acc[1][3][3]};
        STAMP(3 + 8 * L);
        BAR();
        if (L == 0) for (int i = 0; i < nrole; ++i) BAR();
    }
}

__device__ __forceinline__ f32x2 vecsum(rsrc_t rw, int base, int G, int g, bool needx, int xoff, u32& xd) {
    int off[4];
#pragma unroll
    for (int i = 0; i < 4; ++i) off[i] = base + (g + 4 * i) * 1024 + G * 16;
    u32x4 v[4];
    gather<4>(rw, off, v, needx, xoff, xd);
    float a[8];
#pragma unroll
    for (int s = 0; s < 8; ++s) a[s] = 0.f;
#pragma unroll
    for (int i = 0; i < 4; ++i) {
        const float sc = hi_f(v[i].w);
        a[0] = fmaf(h_lo(v[i].x), sc, a[0]); a[1] = fmaf(h_hi(v[i].x), sc, a[1]);
        a[2] = fmaf(h_lo(v[i].y), sc, a[2]); a[3] = fmaf(h_hi(v[i].y), sc, a[3]);
        a[4] = fmaf(h_lo(v[i].z), sc, a[4]); a[5] = fmaf(h_hi(v[i].z), sc, a[5]);
        a[6] = fmaf(h_lo(v[i].w), sc, a[6]);
    }
    const float u0 = tr32(a[0], a[4]), u1 = tr32(a[1], a[5]), u2 = tr32(a[2], a[6]), u3 = tr32(a[3], a[7]);
    return f32x2{tr16(u0, u2), tr16(u1, u3)};
}

__device__ __forceinline__ void wave_stats(char* sm, u32 xp, int g, int rho, int l) {
    const float xa = lo_f(xp), xb = hi_f(xp);
    const float piv = asf(__builtin_amdgcn_readfirstlane(asu(xa)));
    const float da = xa - piv, db = (g == 3) ? 0.f : xb - piv;
    float z = tr32(da + db, fmaf(da, da, db * db));
    z = sum16(z);
    z += dppf<ROR1>(z); z += dppf<ROR2>(z); z += dppf<ROR4>(z); z += dppf<ROR8>(z);
    const float S1 = asf(__builtin_amdgcn_readlane(asu(z), 0)), S2 = asf(__builtin_amdgcn_readlane(asu(z), 32));
    const float t = S1 * (1.0f / 112.0f);
    if (l == 0) { lds<float>(sm, S_STAT + rho * 4) = piv + t; lds<float>(sm, S_STAT + 16 + rho * 4) = fmaf(-t, S1, S2); }
}

template <bool TWICE>
__device__ __forceinline__ void ln_coef(char* sm, float& mu, float& R) {
    const f32x4 mw = lds<f32x4>(sm, S_STAT), m2 = lds<f32x4>(sm, S_STAT + 16);
    const float mean = 0.25f * ((mw[0] + mw[1]) + (mw[2] + mw[3]));
    const float d0 = mw[0] - mean, d1 = mw[1] - mean, d2 = mw[2] - mean, d3 = mw[3] - mean;
    const float dd = fmaf(d0, d0, d1 * d1) + fmaf(d2, d2, d3 * d3);
    const float var = fmaxf(fmaf(dd, 112.0f, (m2[0] + m2[1]) + (m2[2] + m2[3])) * (1.0f / 448.0f), 0.f);
    const float mh = rbf(mean);
    const float dh = rbf(__builtin_amdgcn_sqrtf(rbf(rbf(var) + 1e-5f)));
    const float rd = __builtin_amdgcn_rcpf(dh);
    if (!TWICE) { mu = mh; R = rd; return; }
    const float mh2 = rbf((mean - mh) * rd);
    const float dh2 = rbf(__builtin_amdgcn_sqrtf(rbf(rbf(var * rd * rd) + 1e-5f)));
    mu = fmaf(mh2, dh, mh);
    R = rd * __builtin_amdgcn_rcpf(dh2);
}

template <int ROLE>
__device__ __forceinline__ void r_wave(const Params& P, char* sm, int h, int c, int rho, int l, u32 tok0, u32 tok1, u32 t0) {
    const int n = l & 15, g = l >> 4;
    rsrc_t rw = mkrs(P.ws, WS_TOTAL);
    const int win = 14 * n - 2 * (n & 1);
    const bool odd = n & 1;
    constexpr int NB8 = 2, NB7 = 6;
    u32x4 W8[NB8][8], W7[NB7][7], WQ[4][4];
    const int G = 16 * rho + n;
    u32 xd = 0, bias = 0;
    int cA = 0, cB = 0;
    const int qh = 2 * h + (c & 1), cb = c >> 1;
    const int sl = 2 * h + ((c - 8) >> 1), hf = (c - 8) & 1;
    const int beta = rho >> 1, kh = rho & 1;
    if (ROLE == 2 || ROLE == 3) {
        cA = colmap(G, 2 * g); cB = (g == 3) ? 448 : colmap(G, 2 * g + 1);
    }
    if (ROLE == 2) {
        rsrc_t rx = mkrs(P.x, 896);
        xd = ld16(rx, cA * 2) | (ld16(rx, cB * 2) << 16);
        rsrc_t rb = mkrs(P.b1, 3584);
        bias = ld16(rb, (l & 7) == 7 ? 4096 : (112 * sl + colmap(8 * kh + (l >> 3), l & 7)) * 2);
    }
    if (ROLE == 3) {
        rsrc_t rb = mkrs(P.b2, 896);
        bias = ld16(rb, cA * 2) | (ld16(rb, cB * 2) << 16);
    }
    u32 pf0 = 0, pf1 = 0;
    const int xa_w = S_XA + rho * 256 + n * 16 + g * 4;
    const int xa_r = (n < 8) ? S_XA + rho * 256 + g * 16 : S_ONES;
    auto pfw = [&]() {
            if (ROLE == 0 || ROLE == 3) {
                const int wi = (c - 12) * 4 + rho;
                if (wi < 56) { if (!OWT) pf0 = __builtin_amdgcn_raw_buffer_load_b32(mkrs(P.wo, 4096 * 896), h * 458752 + (wi * 64 + l) * 128, 0, 0); }
                else pf0 = __builtin_amdgcn_raw_buffer_load_b32(mkrs(P.w2, 1792 * 896), h * 200704 + ((wi - 56) * 64 + l) * 128, 0, 0);
                if (wi < 35) { const int i = wi * 64 + l; pf1 = __builtin_amdgcn_raw_buffer_load_b32(mkrs(P.w1, 448 * 3584), (i / 5) * 3584 + ((448 * h) & ~127) + (i % 5) * 128, 0, 0); }
                else if (wi == 35) pf1 = __builtin_amdgcn_raw_buffer_load_b32(mkrs(P.w2, 1792 * 896), h * 200704 + (1536 + (l & 31)) * 128, 0, 0);
            }
    };
    auto owl = [&]() {
            if (ROLE == 1) {
                rsrc_t r = mkrs(P.wo, 4096 * 896);
                const int v = (qh * 256 + 8 * (8 * rho + g)) * 896 + cb * 224 + win;
#pragma unroll
                for (int k = 0; k < 2; ++k)
#pragma unroll
                    for (int j = 0; j < 8; ++j) W8[k][j] = ld128(r, v, (32 * k + 2 * (j & 3) + (j >> 2)) * 896, NT);
            }
    };
    constexpr int EARLY = ROLE == 1 ? OWT : (ROLE == 0 || ROLE == 3) ? PFE : 0;
    if (EARLY) {
        while (true) { MEMBAR; if (__builtin_amdgcn_readfirstlane(lds<u32>(sm, S_FLAG + 16)) >= EARLY) break; __builtin_amdgcn_s_sleep(1); }
        MEMBAR;
        if (ROLE == 1) { owl(); prep7<8>(W8[0], odd); prep7<8>(W8[1], odd); } else pfw();
        __builtin_amdgcn_sched_barrier(0);
    }
    STAMP(4);
#pragma unroll 1
    for (int L = 0; L < 2; ++L) {
        const int lb = WS_L + L * L_SIZE;
        BAR();
        STAMP(5 + 8 * L);
        {
            const int hd = rho >> 1, gam = 16 * (rho & 1) + (l >> 2), pr = l & 3;
            const f32x4 m4 = lds<f32x4>(sm, S_M + hd * 16), l4 = lds<f32x4>(sm, S_M + 32 + hd * 16);
            const u32x4 e4 = lds<u32x4>(sm, S_M + 64);
            f32x2 pv[4];
#pragma unroll
            for (int w = 0; w < 4; ++w) pv[w] = lds<f32x2>(sm, S_P + w * 2048 + hd * 1024 + gam * 32 + pr * 8);
            const float M = fmaxf(fmaxf(m4[0], m4[1]), fmaxf(m4[2], m4[3]));
            const u32 ev = umax(umax(umax(e4[0], e4[1]), umax(e4[2], e4[3])), 8u);
            const float sinv = asf((261u - ev) << 23);
            float o0 = 0.f, o1 = 0.f, Ls = 0.f;
#pragma unroll
            for (int w = 0; w < 4; ++w) {
                const float e = __builtin_amdgcn_exp2f(m4[w] - M);
                o0 = fmaf(e, pv[w][0], o0); o1 = fmaf(e, pv[w][1], o1); Ls = fmaf(e, l4[w], Ls);
            }
            const u32x4 gr = quad_gather(san(pkf16(o0 * sinv, o1 * sinv)));
            if (pr == 0) st128(rw, lb + L_OB + (2 * h + hd) * 16384 + c * 512 + gam * 16, gr);
            if (l == 1 && (rho & 1) == 0) st128(rw, lb + L_ML + ((2 * h + hd) * 32 + c) * 16, u32x4{san(asu(M)), san(asu(Ls)), (ev - 7u) << 23, 0u});
        }
        STAMP(6 + 8 * L);
        if (L == 0) {
            if (ROLE == 0 || ROLE == 3) TGATE(TGP);
            if (!PFE) pfw();
            const int tko = WS_TOK + ((l & 7) * 32 + (ROLE == 2 ? (l >> 3) : 8 + ((l >> 3) & 3))) * 64;
            u32x2 tkv = {0, 0};
            if (ROLE == 2 || ROLE == 3) tkv = __builtin_amdgcn_raw_buffer_load_b64(rw, tko, 0, SC1);
            if (ROLE == 1) TGATE(TGO);
            if (ROLE == 2) TGATE(TGM);
            if (ROLE == 3) TGATE(TGQ);
            if (!OWT) owl();
            if (ROLE == 2 && MWD) __builtin_amdgcn_s_sleep(MWD);
            if (ROLE == 2) {
                rsrc_t r1 = mkrs(P.w1, 448 * 3584);
                const int rb1 = 7 * (16 * rho + g);
                const int v1 = (rb1 - (g & 1)) * 3584 + sl * 224 + win, v0 = (rb1 + 6 * (g & 1)) * 3584 + sl * 224 + win;
#pragma unroll
                for (int k = 0; k < 4; ++k)
#pragma unroll
                    for (int j = 0; j < 7; ++j) W7[k][j] = ld128(r1, j ? v1 : v0, (28 * k + j) * 3584, NT);
                rsrc_t r2 = mkrs(P.w2, 1792 * 896);
                const int rb2 = 112 * sl + 7 * (8 * kh + g);
                const int cofs = hf * 448 + beta * 224 + win;
                const int u1 = (rb2 - (g & 1)) * 896 + cofs, u0 = (rb2 + 6 * (g & 1)) * 896 + cofs;
#pragma unroll
                for (int k = 0; k < 2; ++k)
#pragma unroll
                    for (int j = 0; j < 7; ++j) W7[NB7 - 2 + k][j] = ld128(r2, j ? u1 : u0, (28 * k + j) * 896, NT);
            }
            if (ROLE == 3) {
                if (QWD) __builtin_amdgcn_s_sleep(QWD);
                rsrc_t r = mkrs(P.wq, 448 * 8192);
                const int rb1 = 7 * (16 * rho + g);
                const int cofs = (512 * h + 64 * (c - 12)) * 2 + 8 * n;
                const int v1 = (rb1 - (g & 1)) * 8192 + cofs, v0 = (rb1 + 6 * (g & 1)) * 8192 + cofs;
#pragma unroll
                for (int k = 0; k < 4; ++k)
#pragma unroll
                    for (int j = 0; j < 8; ++j) {
                        u32x2 w = {0, 0};
                        if (j < 7) w = __builtin_amdgcn_raw_buffer_load_b64(r, j ? v1 : v0, (28 * k + j) * 8192, NT);
                        WQ[k][j >> 1][2 * (j & 1)] = w.x; WQ[k][j >> 1][2 * (j & 1) + 1] = w.y;
                    }
            }
            STAMP(12);
            if (ROLE == 1 && !OWT) { prep7<8>(W8[0], odd); prep7<8>(W8[1], odd); }
            if (ROLE == 2) {
#pragma unroll
                for (int k = 0; k < NB7; ++k) prep7<7>(W7[k], odd);
            }
            if (ROLE == 3) {
#pragma unroll
                for (int k = 0; k < 4; ++k) {
                    u32x4 B[4];
#pragma unroll
                    for (int jj = 0; jj < 4; ++jj)
#pragma unroll
                        for (int e = 0; e < 4; ++e) B[e][jj] = __builtin_amdgcn_perm(WQ[k][jj][2 + (e >> 1)], WQ[k][jj][e >> 1], (e & 1) ? SEL_HI : SEL_LO);
#pragma unroll
                    for (int e = 0; e < 4; ++e) WQ[k][e] = B[e];
                }
            }
            if (ROLE == 2 || ROLE == 3) {
                int spins = 0;
                while (!__all((tkv.x == tok0) & (tkv.y == tok1)) && ++spins <= SPIN_MAX) {
                    MEMBAR;
                    tkv = __builtin_amdgcn_raw_buffer_load_b64(rw, tko, 0, SC1);
                }
                MEMBAR;
            }
        }
        STAMP(7 + 8 * L);
        if (ROLE == 1) {
            int off[8];
#pragma unroll
            for (int i = 0; i < 4; ++i) {
                off[i] = lb + L_ML + (qh * 32 + 8 * i + (n >> 1)) * 16;
                off[4 + i] = lb + L_OB + qh * 16384 + (8 * i + (n >> 1)) * 512 + rho * 128 + (n & 1) * 64 + g * 16;
            }
            u32x4 v[8];
            gather<8>(rw, off, v, false, 0, xd);
            STAMP(8 + 8 * L);
            float Mx = fmaxf(fmaxf(asf(v[0].x), asf(v[1].x)), fmaxf(asf(v[2].x), asf(v[3].x)));
            Mx = fmaxf(Mx, dppf<ROR2>(Mx)); Mx = fmaxf(Mx, dppf<ROR4>(Mx)); Mx = fmaxf(Mx, dppf<ROR8>(Mx));
            float a[8], Lp = 0.f;
#pragma unroll
            for (int s = 0; s < 8; ++s) a[s] = 0.f;
#pragma unroll
            for (int i = 0; i < 4; ++i) {
                const float e = __builtin_amdgcn_exp2f(asf(v[i].x) - Mx);
                Lp = fmaf(e, asf(v[i].y), Lp);
                const float w = e * asf(v[i].z);
#pragma unroll
                for (int s = 0; s < 4; ++s) { a[2 * s] = fmaf(h_lo(v[4 + i][s]), w, a[2 * s]); a[2 * s + 1] = fmaf(h_hi(v[4 + i][s]), w, a[2 * s + 1]); }
            }
            const u32x4 A = {pkbf(a[0], a[1]), pkbf(a[2], a[3]), pkbf(a[4], a[5]), pkbf(a[6], a[7])};
            float t[8];
#pragma unroll
            for (int e = 0; e < 7; ++e) {
                const f32x4 d0 = mfma(A, W8[0][e], f32x4{0, 0, 0, 0}), d1 = mfma(A, W8[1][e], f32x4{0, 0, 0, 0});
                t[e] = (d0[0] + d0[2]) + (d1[1] + d1[3]);
            }
            t[7] = 0.f;
            Lp += dppf<ROR2>(Lp); Lp += dppf<ROR4>(Lp); Lp += dppf<ROR8>(Lp);
            const float il = __builtin_amdgcn_rcpf(Lp);
            const float u0 = tr32(t[0], t[4]), u1 = tr32(t[1], t[5]), u2 = tr32(t[2], t[6]), u3 = tr32(t[3], t[7]);
            lds<f32x2>(sm, S_PART + rho * 512 + n * 32 + g * 8) = f32x2{tr16(u0, u2) * il, tr16(u1, u3) * il};
            BAR();
            if (rho == 0) {
                f32x2 s = lds<f32x2>(sm, S_PART + l * 8) + lds<f32x2>(sm, S_PART + 512 + l * 8) + (lds<f32x2>(sm, S_PART + 1024 + l * 8) + lds<f32x2>(sm, S_PART + 1536 + l * 8));
                const u32x4 gr = make_granule(s[0], s[1], l & 3);
                if ((l & 3) == 0) st128(rw, lb + L_C + qh * 1024 + (16 * cb + (l >> 2)) * 16, gr);
            }
        }
        if (ROLE == 2) {
            const f32x2 s = vecsum(rw, lb + L_C, G, g, L == 1, WS_X2 + G * 16 + g * 4, xd);
            STAMP(8 + 8 * L);
            const u32 sb = pkbf(s[0], s[1]);
            const u32 xp = pkbf(lo_f(xd) + lo_f(sb), hi_f(xd) + hi_f(sb));
            lds<u32>(sm, xa_w) = xp;
            WAVE_SYNC();
            f32x4 acc[7];
#pragma unroll
            for (int e = 0; e < 7; ++e) acc[e] = f32x4{0, 0, 0, 0};
#pragma unroll
            for (int k = 0; k < 4; ++k) {
                const u32x4 A = lds<u32x4>(sm, xa_r + 64 * k);
#pragma unroll
                for (int e = 0; e < 7; ++e) acc[e] = mfma(A, W7[k][e], acc[e]);
            }
            wave_stats(sm, xp, g, rho, l);
            if (c == 8 && h == 0 && l < 16) {
                u32x4 xg = lds<u32x4>(sm, S_XA + rho * 256 + l * 16);
                st128(rw, lb + L_X1 + (16 * rho + l) * 16, u32x4{san(xg.x), san(xg.y), san(xg.z), san(xg.w)});
            }
            lds<f32x4>(sm, S_PART + rho * 1024 + (g >> 1) * 512 + n * 32 + (g & 1) * 16) =
                (g & 1) ? f32x4{acc[4][0], acc[5][0], acc[6][0], 0.f} : f32x4{acc[0][0], acc[1][0], acc[2][0], acc[3][0]};
            BAR();
            STAMP(9 + 8 * L);
            {
                const int pa = S_PART + (64 * kh + l) * 4;
                const float ps = (lds<float>(sm, pa) + lds<float>(sm, pa + 1024)) + (lds<float>(sm, pa + 2048) + lds<float>(sm, pa + 3072));
                const float ss = (lds<float>(sm, pa + 512) + lds<float>(sm, pa + 1536)) + (lds<float>(sm, pa + 2560) + lds<float>(sm, pa + 3584));
                float mu, R;
                ln_coef<true>(sm, mu, R);
                const float u = rbf(rbf(R * fmaf(-mu, ss, ps)) + lo_f(bias));
                const float y = u * fmaf(u * u, 0.044715f, 1.0f) * (-2.0f * 0.7978845608028654f * 1.4426950408889634f);
                const float hv = u * __builtin_amdgcn_rcpf(1.0f + __builtin_amdgcn_exp2f(y));
                lds<u16>(sm, S_HA + rho * 128 + l * 2) = ((l & 7) == 7) ? (u16)0 : (u16)pkbf(hv, hv);
            }
            WAVE_SYNC();
#pragma unroll
            for (int e = 0; e < 7; ++e) acc[e] = f32x4{0, 0, 0, 0};
#pragma unroll
            for (int k = 0; k < 2; ++k) {
                const u32x4 A = lds<u32x4>(sm, S_HA + rho * 128 + 64 * k + g * 16);
#pragma unroll
                for (int e = 0; e < 7; ++e) acc[e] = mfma(A, W7[NB7 - 2 + k][e], acc[e]);
            }
            if (g < 2)
                lds<f32x4>(sm, S_PART2 + rho * 512 + n * 32 + (g & 1) * 16) =
                    (g & 1) ? f32x4{acc[4][0], acc[5][0], acc[6][0], 0.f} : f32x4{acc[0][0], acc[1][0], acc[2][0], acc[3][0]};
            BAR();
            if (rho < 2) {
                f32x2 s2 = lds<f32x2>(sm, S_PART2 + rho * 1024 + l * 8) + lds<f32x2>(sm, S_PART2 + rho * 1024 + 512 + l * 8);
                const u32x4 gr = make_granule(s2[0], s2[1], l & 3);
                if ((l & 3) == 0) st128(rw, lb + L_E + sl * 1024 + (32 * hf + 16 * rho + (l >> 2)) * 16, gr);
            }
        }
        if (ROLE == 3) {
            if (L == 1 && !(c == 12 && h == 0)) break;
            const f32x2 s = vecsum(rw, lb + L_E, G, g, true, lb + L_X1 + G * 16 + g * 4, xd);
            STAMP(8 + 8 * L);
            const u32 sb = pkbf(s[0], s[1]);
            const u32 sc = pkbf(lo_f(sb) + lo_f(bias), hi_f(sb) + hi_f(bias));
            const u32 xp = pkbf(lo_f(xd) + lo_f(sc), hi_f(xd) + hi_f(sc));
            if (L == 1) {
                u16* o = (u16*)P.out;
                o[cA] = (u16)xp;
                if (g != 3) o[cB] = (u16)(xp >> 16);
                STAMP(10 + 8 * L);
                break;
            }
            lds<u32>(sm, xa_w) = xp;
            WAVE_SYNC();
            f32x4 acc[4];
#pragma unroll
            for (int e = 0; e < 4; ++e) acc[e] = f32x4{0, 0, 0, 0};
#pragma unroll
            for (int k = 0; k < 4; ++k) {
                const u32x4 A = lds<u32x4>(sm, xa_r + 64 * k);
#pragma unroll
                for (int e = 0; e < 4; ++e) acc[e] = mfma(A, WQ[k][e], acc[e]);
            }
            wave_stats(sm, xp, g, rho, l);
            if (c == 12 && h == 0 && l < 16) {
                u32x4 xg = lds<u32x4>(sm, S_XA + rho * 256 + l * 16);
                st128(rw, WS_X2 + (16 * rho + l) * 16, u32x4{san(xg.x), san(xg.y), san(xg.z), san(xg.w)});
            }
            if ((g & 1) == 0) lds<f32x4>(sm, S_PART + rho * 512 + (g >> 1) * 256 + n * 16) = f32x4{acc[0][0], acc[1][0], acc[2][0], acc[3][0]};
            BAR();
            if (rho == 0) {
                const int pa = S_PART + (l & 31) * 8;
                const f32x2 ps = (lds<f32x2>(sm, pa) + lds<f32x2>(sm, pa + 512)) + (lds<f32x2>(sm, pa + 1024) + lds<f32x2>(sm, pa + 1536));
                const f32x2 ss = (lds<f32x2>(sm, pa + 256) + lds<f32x2>(sm, pa + 768)) + (lds<f32x2>(sm, pa + 1280) + lds<f32x2>(sm, pa + 1792));
                float mu, R;
                ln_coef<false>(sm, mu, R);
                const u32x4 gr = quad_gather(san(pkbf(R * fmaf(-mu, ss[0], ps[0]), R * fmaf(-mu, ss[1], ps[1]))));
                if ((l & 3) == 0 && l < 32) st128(rw, WS_Q2 + (512 * h + 64 * (c - 12)) * 2 + (l >> 2) * 16, gr);
            }
        }
        STAMP(10 + 8 * L);
        if (L == 1) break;
        if (rho == 3) {
            int off[1] = {WS_Q2 + h * 1024 + l * 16};
            u32x4 v[1];
            gather<1>(rw, off, v, false, 0, xd);
            lds<u32x4>(sm, S_Q + (l >> 5) * 640 + (l & 31) * 16) = v[0];
            if (h == 0 && c == 0 && l == 0) st128(rw, WS_EPOCH, u32x4{tok0, tok1, 0u, 0u});
        }
        asm volatile("" :: "v"(pf0), "v"(pf1));
        STAMP(11);
        BAR();
    }
}

__global__ __launch_bounds__(512, 2) void mega(Params P) {
    __shared__ __attribute__((aligned(16))) char sm[S_TOTAL + STAMP_LDS];
    const int t = threadIdx.x, l = t & 63, wv = __builtin_amdgcn_readfirstlane(t >> 6);
    const int b = blockIdx.x, h = b & 7, c = b >> 3;
    const int role = c < 8 ? 1 : c < 12 ? 2 : c < 20 ? 3 : 0;
    u32 cpf = 0;
    if (CPF) {
        u64 pc;
        asm volatile("s_getpc_b64 %0" : "=s"(pc));
        const int line = ((c & 7) * 8 + wv) * 4 + (l & 3);
        cpf = *(const __attribute__((address_space(1))) u32*)((pc & ~255ull) + (u32)(line < CPF ? line : CPF - 1) * 128u);
        __builtin_amdgcn_sched_barrier(0);
    }
    if (wv >= 4) {
        a_wave(P, sm, h, c, wv - 4, l, role == 2 ? 2 : role == 0 ? 0 : 1);
        asm volatile("" :: "v"(cpf));
        STAMP_DUMP();
        return;
    }
    __builtin_amdgcn_s_setprio(3);
    const u32 t0 = (TGP || TGO || TGM || TGQ) ? (u32)__builtin_amdgcn_s_memrealtime() : 0u;
    const int rho = wv, tt = t;
    rsrc_t rw = mkrs(P.ws, WS_TOTAL);
    STAMP(0);
    rsrc_t rx = mkrs(P.x, 896), rq = mkrs(P.wq, 448 * 8192);
    const int cq = c;
    u16 xsr[7], xrr[4];
    u32x4 wr[4];
    u32x2 ep;
    auto xl = [&]() {
        ep = __builtin_amdgcn_raw_buffer_load_b64(rw, WS_EPOCH, 0, 0);
#pragma unroll
        for (int i = 0; i < 7; ++i) xsr[i] = __builtin_amdgcn_raw_buffer_load_b16(rx, (l + 64 * i) * 2, 0, 0);
#pragma unroll
        for (int i = 0; i < 4; ++i) xrr[i] = __builtin_amdgcn_raw_buffer_load_b16(rx, ((tt >> 1) + 128 * i) * 2, 0, 0);
        if (XFIRST) __builtin_amdgcn_sched_barrier(0);
    };
    if (XFIRST == 1) xl();
    const u32 pfq = __builtin_amdgcn_raw_buffer_load_b32(rq, tt < 112 ? (14 * c + (tt >> 3)) * 8192 + h * 1024 + (tt & 7) * 128 : 0x7ffffff0, 0, 0);
    if (XFIRST == 2) { __builtin_amdgcn_sched_barrier(0); xl(); }
#pragma unroll
    for (int i = 0; i < 4; ++i) wr[i] = ld128(rq, ((tt >> 1) + 128 * i) * 8192 + (512 * h + 16 * cq + 8 * (tt & 1)) * 2, 0, 0);
    if (XFIRST == 0) xl();
    if (rho == 2) {
        const u32x4 S4 = {SENT, SENT, SENT, SENT};
        for (int L = 0; L < 2; ++L) st128(rw, WS_L + L * L_SIZE + L_OB + (2 * h + (l >> 5)) * 16384 + c * 512 + (l & 31) * 16, S4);
        if (l < 4) st128(rw, WS_L + (l >> 1) * L_SIZE + L_ML + ((2 * h + (l & 1)) * 32 + c) * 16, S4);
        if (role == 1 && l < 32) st128(rw, WS_L + (l >> 4) * L_SIZE + L_C + (2 * h + (c & 1)) * 1024 + (16 * (c >> 1) + (l & 15)) * 16, S4);
        if (role == 2) {
            st128(rw, WS_L + (l >> 5) * L_SIZE + L_E + (2 * h + ((c - 8) >> 1)) * 1024 + (32 * ((c - 8) & 1) + (l & 31)) * 16, S4);
            if (c == 8 && h == 0) for (int L = 0; L < 2; ++L) st128(rw, WS_L + L * L_SIZE + L_X1 + l * 16, S4);
        }
        if (role == 3) {
            if (l < 8) st128(rw, WS_Q2 + (512 * h + 64 * (c - 12)) * 2 + l * 16, S4);
            if (c == 12 && h == 0) st128(rw, WS_X2 + l * 16, S4);
        }
    }
    if (rho == 0) {
        if (l < 32) lds<u32x4>(sm, S_ZERO + l * 16) = u32x4{0, 0, 0, 0};
        else if (l < 48) lds<u32x4>(sm, S_ONES + (l - 32) * 16) = u32x4{0x3F803F80u, 0x3F803F80u, 0x3F803F80u, 0x3F803F80u};
        else if (l < 50) lds<u32x4>(sm, S_FLAG + (l - 48) * 16) = u32x4{0, 0, 0, 0};
    }
    STAMP(19);
    BAR();
    const u64 tk = (((u64)ep.y << 32) | ep.x) + 1;
    const u32 tok0 = (u32)tk, tok1 = (u32)(tk >> 32);
    float xs[7], xr[4];
#pragma unroll
    for (int i = 0; i < 7; ++i) xs[i] = lo_f(xsr[i]);
#pragma unroll
    for (int i = 0; i < 4; ++i) xr[i] = lo_f(xrr[i]);
    {
        float s = 0.f;
#pragma unroll
        for (int i = 0; i < 7; ++i) s += xs[i];
        const float mean = wave_sum(s) * (1.0f / 448.0f);
        float v = 0.f;
#pragma unroll
        for (int i = 0; i < 7; ++i) { const float d = xs[i] - mean; v = fmaf(d, d, v); }
        const float var = wave_sum(v) * (1.0f / 448.0f);
        const float mh = rbf(mean), dh = rbf(sqrtf(rbf(rbf(var) + 1e-5f)));
        float a[8];
#pragma unroll
        for (int k = 0; k < 8; ++k) a[k] = 0.f;
#pragma unroll
        for (int i = 0; i < 4; ++i) {
            const float y = rbf(rbf(xr[i] - mh) / dh);
#pragma unroll
            for (int k = 0; k < 4; ++k) { a[2 * k] = fmaf(y, lo_f(wr[i][k]), a[2 * k]); a[2 * k + 1] = fmaf(y, hi_f(wr[i][k]), a[2 * k + 1]); }
        }
        const float u0 = tr32(a[0], a[4]), u1 = tr32(a[1], a[5]), u2 = tr32(a[2], a[6]), u3 = tr32(a[3], a[7]);
        float z0 = tr16(u0, u2), z1 = tr16(u1, u3);
        z0 += dppf<ROR2>(z0); z1 += dppf<ROR2>(z1); z0 += dppf<ROR4>(z0); z1 += dppf<ROR4>(z1); z0 += dppf<ROR8>(z0); z1 += dppf<ROR8>(z1);
        if ((l & 15) < 2) lds<f32x2>(sm, S_P + rho * 64 + (l & 1) * 32 + (l >> 4) * 8) = f32x2{z0, z1};
        asm volatile("" :: "v"(pfq), "v"(cpf));
    }
    STAMP(1);
    asm volatile("s_waitcnt vmcnt(0)" ::: "memory");
    if (rho == 2) STAMP(2);
    if (rho != 3) { if (l == 0) __hip_atomic_fetch_add((u32*)(sm + S_FLAG + 4), 1u, __ATOMIC_RELAXED, __HIP_MEMORY_SCOPE_WORKGROUP); }
    else {
        while (true) {
            MEMBAR;
            if (__builtin_amdgcn_readfirstlane(lds<u32>(sm, S_FLAG + 4)) == 3) break;
        }
        MEMBAR;
        const int pa = S_P + (l & 15) * 4;
        const float s = (lds<float>(sm, pa) + lds<float>(sm, pa + 64)) + (lds<float>(sm, pa + 128) + lds<float>(sm, pa + 192));
        const u32 d0 = pkbf(s, dppf<0x55>(s)), d1 = pkbf(dppf<0xAA>(s), dppf<0xFF>(s));
        if (l < 16 && (l & 3) == 0) st128(rw, WS_TOK + (h * 32 + cq) * 64 + (l >> 2) * 16, u32x4{tok0, tok1, d0, d1});
        MEMBAR;
        if (l == 0) lds<u32>(sm, S_FLAG + 8) = 1u;
        MEMBAR;
        STAMP(2);
    }
    if (rho == 3 || rho < NPOLL) {
        if (rho == 0) __builtin_amdgcn_s_sleep(PDEL0);
        if (rho == 1) __builtin_amdgcn_s_sleep(PDEL1);
        if (rho == 3) __builtin_amdgcn_s_sleep(QSLEEP);
        u32x4 v0 = {0, 0, 0, 0}, v1 = {0, 0, 0, 0};
        bool ok0 = false, ok1 = false;
        int spins = 0;
        while (true) {
            MEMBAR;
            if (__builtin_amdgcn_readfirstlane(lds<u32>(sm, S_FLAG)) != 0) break;
            if (!ok0) v0 = ld128(rw, WS_TOK + h * 2048 + l * 16, 0, SC1);
            if (!ok1) v1 = ld128(rw, WS_TOK + h * 2048 + 1024 + l * 16, 0, SC1);
            if (B2 < 32) { MEMBAR; if (l == 0) lds<u32>(sm, S_FLAG + 12) = 1u; MEMBAR; __builtin_amdgcn_sched_barrier(0); }
            ok0 = (v0.x == tok0) & (v0.y == tok1); ok1 = (v1.x == tok0) & (v1.y == tok1);
            if (__all(ok0 & ok1) || ++spins > SPIN_MAX) {
                lds<u32x2>(sm, S_Q + l * 8) = u32x2{v0.z, v0.w};
                lds<u32x2>(sm, S_Q + 640 + l * 8) = u32x2{v1.z, v1.w};
                MEMBAR;
                if (l == 0) lds<u32>(sm, S_FLAG) = 1u;
                MEMBAR;
                break;
            }
        }
    }
    STAMP(3);
    if (role == 1) r_wave<1>(P, sm, h, c, rho, l, tok0, tok1, t0);
    else if (role == 2) r_wave<2>(P, sm, h, c, rho, l, tok0, tok1, t0);
    else if (role == 3) r_wave<3>(P, sm, h, c, rho, l, tok0, tok1, t0);
    else r_wave<0>(P, sm, h, c, rho, l, tok0, tok1, t0);
    STAMP_DUMP();
}

}

extern "C" void kernel_launch(void* const* d_in, const int* in_sizes, int n_in, void* d_out, int out_size, void* d_ws,
                              size_t ws_size, hipStream_t stream) {
    mk::Params P;
    P.x = (const char*)d_in[0]; P.kc = (const char*)d_in[1]; P.vc = (const char*)d_in[2];
    P.wq = (const char*)d_in[3]; P.wo = (const char*)d_in[4]; P.w1 = (const char*)d_in[6];
    P.b1 = (const char*)d_in[7]; P.w2 = (const char*)d_in[8]; P.b2 = (const char*)d_in[9];
    P.out = (char*)d_out; P.ws = (char*)d_ws;
    mk::mega<<<256, 512, 0, stream>>>(P);
}
```
